```python
import torch

def _softmax(x, axis=-1):
    return torch.softmax(x, dim=axis)

def _gelu(x):
    return torch.nn.functional.gelu(x, approximate='tanh')

def _silu(x):
    return torch.nn.functional.silu(x)

def _sigmoid(x):
    return torch.sigmoid(x)

def _layernorm(x, axis=-1, eps=1e-05):
    m = x.mean(dim=axis, keepdim=True)
    v = x.var(dim=axis, keepdim=True, unbiased=False)
    return (x - m) / torch.sqrt(v + eps)

def _scatter_add(tgt, idx, src, axis=0):
    return tgt.clone().index_add_(axis, idx, src)

def _segment_sum(src, seg, n):
    out = torch.zeros((n,) + src.shape[1:], dtype=src.dtype)
    return out.index_add_(0, seg, src)

def _pad(x, pw):
    flat = tuple((v for pair in reversed(pw) for v in pair))
    return torch.nn.functional.pad(x, flat)
kernel_name = 'standalone__window_attn_twins__vision'

def setup_inputs():

    _w = lambda a: (lambda t: t.to(torch.bfloat16) if t.is_floating_point() else t)(torch.as_tensor(a))
    H = W = 224 // 7 * 7
    C = 96
    return {'x': _w((lambda *s: torch.randn(*s, dtype=torch.bfloat16) * 0.8)(1, H, W, C)), 'wq': _w((lambda *s: torch.randn(*s, dtype=torch.bfloat16) * 0.8)(C, C)), 'wkv': _w((lambda *s: torch.randn(*s, dtype=torch.bfloat16) * 0.8)(C, 2 * C)), 'wo': _w((lambda *s: torch.randn(*s, dtype=torch.bfloat16) * 0.05)(C, C))}

def reference(x, wq, wkv, wo):
    B, H, W, C = x.shape
    win = 7
    nh, nw = (H // win, W // win)
    xs = x.reshape(B, nh, win, nw, win, C).mean(axis=(2, 4)).reshape(B, nh * nw, C)
    q = x.reshape(B, H * W, C) @ wq
    kv = xs @ wkv
    k, v = (kv[..., :C].clone(), kv[..., C:].clone())
    scale = C ** (-0.5)
    p = _softmax(torch.einsum('bqc,bkc->bqk', q, k) * scale, axis=-1)
    o = torch.einsum('bqk,bkc->bqc', p, v) @ wo
    return o.reshape(B, H, W, C)
```

```cpp
#include <hip/hip_runtime.h>
#include <hip/hip_bf16.h>
#include <stdint.h>

typedef float    f32x16 __attribute__((ext_vector_type(16)));
typedef float    f32x4  __attribute__((ext_vector_type(4)));
typedef float    f32x2  __attribute__((ext_vector_type(2)));
typedef __bf16   bf16x8 __attribute__((ext_vector_type(8)));
typedef __bf16   bf16x2 __attribute__((ext_vector_type(2)));
typedef unsigned u32x4  __attribute__((ext_vector_type(4)));
typedef unsigned u32x2  __attribute__((ext_vector_type(2)));

constexpr int HH = 224, WW = 224, C = 96, NQ = HH * WW, NK = 1024;

__device__ __forceinline__ float bf2f(unsigned short b) { return __uint_as_float(((unsigned)b) << 16); }
__device__ __forceinline__ unsigned cvtpk(float lo, float hi) { f32x2 v = {lo, hi}; bf16x2 b = __builtin_convertvector(v, bf16x2); return __builtin_bit_cast(unsigned, b); }
__device__ __forceinline__ unsigned short f2bf(float f) { return (unsigned short)(cvtpk(f, 0.f) & 0xffff); }

__global__ __launch_bounds__(256) void prep_kernel(const unsigned short* __restrict__ x, const unsigned short* __restrict__ wq,
                                                   const unsigned short* __restrict__ wkv, const unsigned short* __restrict__ wo,
                                                   unsigned short* __restrict__ Kf, unsigned short* __restrict__ Vf) {
  __shared__ float xs[4][96];
  __shared__ float kv[4][192];
  const int b = blockIdx.x, tid = threadIdx.x;
  const int wr = b >> 3, wc0 = (b & 7) * 4;
  if (tid < 192) {
    const int w = tid / 48, cp = tid % 48;
    float s0 = 0.f, s1 = 0.f;
    for (int i = 0; i < 7; ++i)
      for (int j = 0; j < 7; ++j) {
        unsigned v = *(const unsigned*)(x + ((size_t)(wr * 7 + i) * WW + (wc0 + w) * 7 + j) * C + cp * 2);
        s0 += __uint_as_float(v << 16); s1 += __uint_as_float(v & 0xffff0000u);
      }
    xs[w][cp * 2] = bf2f(f2bf(s0 / 49.f)); xs[w][cp * 2 + 1] = bf2f(f2bf(s1 / 49.f));
  }
  __syncthreads();
  if (tid < 192) {
    float a[4] = {0, 0, 0, 0};
    for (int c = 0; c < 96; ++c) { float wv = bf2f(wkv[c * 192 + tid]);
      for (int w = 0; w < 4; ++w) a[w] += xs[w][c] * wv; }
    for (int w = 0; w < 4; ++w) kv[w][tid] = bf2f(f2bf(a[w]));
  }
  __syncthreads();
  if (tid < 192) {
    float a[4] = {0, 0, 0, 0};
    const int t = b >> 3, r0 = (b & 7) * 4;
    if (tid < 96) {
      const int c = tid;
      for (int n = 0; n < 96; ++n) { float wv = bf2f(wq[c * 96 + n]); for (int w = 0; w < 4; ++w) a[w] += kv[w][n] * wv; }
      const float sc = 0.10206207261596575f * 1.4426950408889634f;
      const int s = c >> 4, h = (c >> 3) & 1, j = c & 7;
      for (int w = 0; w < 4; ++w) Kf[((size_t)(t * 6 + s) * 64 + h * 32 + r0 + w) * 8 + j] = f2bf(a[w] * sc);
    } else {
      const int e = tid - 96;
      for (int n = 0; n < 96; ++n) { float wv = bf2f(wo[n * 96 + e]); for (int w = 0; w < 4; ++w) a[w] += kv[w][96 + n] * wv; }
      const int db = e >> 5, r = e & 31;
      for (int w = 0; w < 4; ++w) { const int key = r0 + w; const int ks = key >> 4, jj = (key >> 3) & 1, h = (key >> 2) & 1, i = key & 3;
        Vf[((size_t)(t * 6 + db * 2 + ks) * 64 + h * 32 + r) * 8 + jj * 4 + i] = f2bf(a[w]); }
    }
  }
}

__global__ __launch_bounds__(256) void attn_kernel(const unsigned short* __restrict__ x, const bf16x8* __restrict__ Kf, const bf16x8* __restrict__ Vf,
                                                   unsigned short* __restrict__ out) {
  const int lane = threadIdx.x & 63, wid = threadIdx.x >> 6, r = lane & 31, h = lane >> 5;
  const int qb = blockIdx.x * 4 + wid;
  if (qb >= NQ / 32) return;
  const int q = qb * 32 + r;
  bf16x8 xf[6];
#pragma unroll
  for (int s = 0; s < 6; ++s) xf[s] = *(const bf16x8*)(x + (size_t)q * C + s * 16 + h * 8);
  f32x16 o[3] = {}; float m = -INFINITY, l = 0.f;
  for (int t = 0; t < 32; ++t) {
    f32x16 sacc = {};
#pragma unroll
    for (int s = 0; s < 6; ++s) sacc = __builtin_amdgcn_mfma_f32_32x32x16_bf16(Kf[(t * 6 + s) * 64 + lane], xf[s], sacc, 0, 0, 0);
    float mx = sacc[0];
#pragma unroll
    for (int i = 1; i < 16; ++i) mx = fmaxf(mx, sacc[i]);
    mx = fmaxf(mx, __shfl_xor(mx, 32));
    const float mn = fmaxf(m, mx), al = exp2f(m - mn); m = mn;
    float ps = 0.f; float p[16];
#pragma unroll
    for (int i = 0; i < 16; ++i) { p[i] = __builtin_amdgcn_exp2f(sacc[i] - mn); ps += p[i]; }
    l = l * al + ps;
#pragma unroll
    for (int d = 0; d < 3; ++d)
#pragma unroll
      for (int i = 0; i < 16; ++i) o[d][i] *= al;
    u32x4 p0 = {cvtpk(p[0], p[1]), cvtpk(p[2], p[3]), cvtpk(p[4], p[5]), cvtpk(p[6], p[7])};
    u32x4 p1 = {cvtpk(p[8], p[9]), cvtpk(p[10], p[11]), cvtpk(p[12], p[13]), cvtpk(p[14], p[15])};
#pragma unroll
    for (int d = 0; d < 3; ++d) {
      o[d] = __builtin_amdgcn_mfma_f32_32x32x16_bf16(Vf[(t * 6 + d * 2 + 0) * 64 + lane], __builtin_bit_cast(bf16x8, p0), o[d], 0, 0, 0);
      o[d] = __builtin_amdgcn_mfma_f32_32x32x16_bf16(Vf[(t * 6 + d * 2 + 1) * 64 + lane], __builtin_bit_cast(bf16x8, p1), o[d], 0, 0, 0);
    }
  }
  l += __shfl_xor(l, 32);
  const float rl = 1.f / l;
#pragma unroll
  for (int d = 0; d < 3; ++d)
#pragma unroll
    for (int g = 0; g < 4; ++g) {
      u32x2 w = {cvtpk(o[d][4 * g] * rl, o[d][4 * g + 1] * rl), cvtpk(o[d][4 * g + 2] * rl, o[d][4 * g + 3] * rl)};
      *(u32x2*)(out + (size_t)q * C + d * 32 + g * 8 + h * 4) = w;
    }
}

extern "C" void kernel_launch(void* const* d_in, const int* in_sizes, int n_in, void* d_out, int out_size, void* d_ws, size_t ws_size, hipStream_t stream) {
  const unsigned short* x = (const unsigned short*)d_in[0];
  const unsigned short* wq = (const unsigned short*)d_in[1];
  const unsigned short* wkv = (const unsigned short*)d_in[2];
  const unsigned short* wo = (const unsigned short*)d_in[3];
  unsigned short* Kf = (unsigned short*)d_ws; unsigned short* Vf = Kf + 1024 * 96;
  prep_kernel<<<256, 256, 0, stream>>>(x, wq, wkv, wo, Kf, Vf);
  attn_kernel<<<392, 256, 0, stream>>>(x, (const bf16x8*)Kf, (const bf16x8*)Vf, (unsigned short*)d_out);
}
```

```cpp
#include <hip/hip_runtime.h>

typedef float    f32x4  __attribute__((ext_vector_type(4)));
typedef float    f32x2  __attribute__((ext_vector_type(2)));
typedef __bf16   bf16x8 __attribute__((ext_vector_type(8)));
typedef __bf16   bf16x2 __attribute__((ext_vector_type(2)));
typedef unsigned u32x4  __attribute__((ext_vector_type(4)));

constexpr int LDS_BYTES = 160 * 1024;

__device__ __forceinline__ unsigned cvtpk(float lo, float hi) { f32x2 v = {lo, hi}; bf16x2 b = __builtin_convertvector(v, bf16x2); return __builtin_bit_cast(unsigned, b); }

__device__ __forceinline__ void ld64_dev(const char* p, bf16x8& a, bf16x8& b, bf16x8& c, bf16x8& d) {
  asm volatile("global_load_dwordx4 %0, %4, off sc1\n\tglobal_load_dwordx4 %1, %4, off offset:1024 sc1\n\t"
               "global_load_dwordx4 %2, %4, off offset:2048 sc1\n\tglobal_load_dwordx4 %3, %4, off offset:3072 sc1\n\ts_waitcnt vmcnt(0)"
               : "=&v"(a), "=&v"(b), "=&v"(c), "=&v"(d) : "v"(p) : "memory");
}

__device__ __forceinline__ void slow_block(const char* x, const char* kv, char* out, unsigned xoff, int lane, int prmask) {
  bf16x8 xf[3];
#pragma unroll
  for (int s = 0; s < 3; ++s) xf[s] = *(const bf16x8*)(x + xoff + s * 64);
  f32x4 o[6] = {}; float m = -INFINITY, l = 0.f;
  for (int tau = 0; tau < 32; ++tau) {
    const char* tp = kv + (tau >> 2) * 57344 + (tau & 3) * 12288 + lane * 16;
    bf16x8 t[12];
#pragma unroll
    for (int i = 0; i < 12; i += 4) ld64_dev(tp + i * 1024, t[i], t[i + 1], t[i + 2], t[i + 3]);
    f32x4 s0 = {}, s1 = {};
#pragma unroll
    for (int s = 0; s < 3; ++s) {
      s0 = __builtin_amdgcn_mfma_f32_16x16x32_bf16(t[s], xf[s], s0, 0, 0, 0);
      s1 = __builtin_amdgcn_mfma_f32_16x16x32_bf16(t[3 + s], xf[s], s1, 0, 0, 0);
    }
    float mx = fmaxf(fmaxf(fmaxf(s0[0], s0[1]), fmaxf(s0[2], s0[3])), fmaxf(fmaxf(s1[0], s1[1]), fmaxf(s1[2], s1[3])));
    mx = fmaxf(mx, __shfl_xor(mx, 16)); mx = fmaxf(mx, __shfl_xor(mx, 32));
    const float mt = fmaxf(m, mx), mn = mt == -INFINITY ? 0.f : mt, al = exp2f(m - mn); m = mt;
    float p[8];
#pragma unroll
    for (int i = 0; i < 4; ++i) { p[i] = exp2f(s0[i] - mn); p[4 + i] = exp2f(s1[i] - mn); }
    l = l * al + (((p[0] + p[1]) + (p[2] + p[3])) + ((p[4] + p[5]) + (p[6] + p[7])));
    const u32x4 pk = {cvtpk(p[0], p[1]), cvtpk(p[2], p[3]), cvtpk(p[4], p[5]), cvtpk(p[6], p[7])};
#pragma unroll
    for (int d = 0; d < 6; ++d) { o[d] *= al; o[d] = __builtin_amdgcn_mfma_f32_16x16x32_bf16(t[6 + d], __builtin_bit_cast(bf16x8, pk), o[d], 0, 0, 0); }
  }
  l += __shfl_xor(l, 16); l += __shfl_xor(l, 32);
  const float rl = 1.f / l;
#pragma unroll
  for (int pr = 0; pr < 3; ++pr) {
    const f32x4 a = o[2 * pr] * rl, b = o[2 * pr + 1] * rl;
    const u32x4 w = {cvtpk(a[0], a[1]), cvtpk(a[2], a[3]), cvtpk(b[0], b[1]), cvtpk(b[2], b[3])};
    if ((prmask >> pr) & 1) *(u32x4*)(out + xoff + pr * 64) = w;
  }
}

__global__ __launch_bounds__(256, 1) void fused_attn(const char* x, char* out, char* ws, const char* wq, const char* wkv, const char* wo) {
  const int b = blockIdx.x; const unsigned tid = threadIdx.x;
  int bad; unsigned x3, x4, x5, x6;
  asm volatile(
      "s_load_dwordx8 s[20:27], %5, 0x0\n\t"
      "s_load_dwordx4 s[28:31], %5, 0x20\n\t"
      "v_lshrrev_b32 v225, 6, %7\n\t"
      "v_and_b32 v223, 63, %7\n\t"
      "s_lshr_b32 s48, %6, 3\n\t"
      "v_readfirstlane_b32 s45, v225\n\t"
      "v_mov_b32 v229, 524288\n\t"
      "s_mul_i32 s48, s48, 301056\n\t"
      "s_and_b32 s49, %6, 7\n\t"
      "s_mul_i32 s49, s49, 5376\n\t"
      "s_add_u32 s55, s48, s49\n\t"
      "s_getpc_b64 s[36:37]\n\t"
      "s_bfe_u32 s48, %6, 0x50003\n\t"
      "s_lshl_b32 s48, s48, 2\n\t"
      "s_add_u32 s48, s48, s45\n\t"
      "s_lshl_b32 s48, s48, 8\n\t"
      "Lcw%=:\n\t"
      "v_lshl_add_u32 v235, v223, 7, s48\n\t"
      "v_min_u32 v234, Lend%=-Lcw%=, v235\n\t"
      "v_add_u32 v225, s48, v235\n\t"
      "v_min_u32 v235, 18304, v235\n\t"
      "v_min_u32 v225, 36480, v225\n\t"
      "s_mov_b64 exec, 3\n\t"
      "global_load_dword v239, v234, s[36:37]\n\t"
      "s_mov_b64 exec, -1\n\t"
      "s_lshl_b32 s48, s45, 6\n\t"
      "v_add_u32 v92, s48, v223\n\t"
      "v_mul_u32_u24 v93, 5462, v92\n\t"
      "v_lshrrev_b32 v93, 18, v93\n\t"
      "s_lshr_b32 s48, %6, 3\n\t"
      "s_mul_i32 s48, s48, 301056\n\t"
      "s_and_b32 s48, %6, 0xf6\n\t"
      "s_and_b32 s49, s48, 7\n\t"
      "s_lshr_b32 s48, s48, 3\n\t"
      "s_mul_i32 s48, s48, 301056\n\t"
      "s_mul_i32 s49, s49, 5376\n\t"
      "s_add_u32 s48, s48, s49\n\t"
      "s_add_u32 s33, s48, 262656\n\t"
      "v_lshrrev_b32 v94, 1, v93\n\t"
      "v_mul_u32_u24 v94, 290304, v94\n\t"
      "v_mul_u32_u24 v93, 4608, v93\n\t"
      "v_add_u32 v93, v93, v94\n\t"
      "v_lshl_add_u32 v92, v92, 4, s33\n\t"
      "v_add_u32 v92, v92, v93\n\t"
      "s_lshl_b32 s48, s45, 10\n\t"
      "s_add_u32 s15, s48, 155392\n\t"
      "s_and_b32 s35, %6, 9\n\t"
      "s_cmp_eq_u32 s45, 3\n\t"
      "s_cselect_b32 s14, 1, s35\n\t"
      "v_mul_u32_u24 v96, 781, v223\n\t"
      "s_mul_i32 s48, s45, 1344\n\t"
      "s_mul_i32 s16, s45, 9408\n\t"
      "s_add_u32 s48, s48, s55\n\t"
      "s_add_u32 s16, s16, 118784\n\t"
      "s_add_u32 s48, s48, 4096\n\t"
      "s_mov_b32 m0, s16\n\t"
      "v_lshl_add_u32 v97, v223, 4, s48\n\t"
      "s_mul_i32 s48, s45, 3211264\n\t"
      "v_mul_u32_u24 v99, 5462, v223\n\t"
      "s_mov_b32 s17, 41664\n\t"
      "v_add_u32 v99, s48, v99\n\t"
      "v_mul_u32_u24 v88, 10923, v223\n\t"
      "s_mul_i32 s48, s45, 96\n\t"
      "s_mul_i32 s36, s45, 9216\n\t"
      "s_add_u32 s48, s48, 4096\n\t"
      "s_add_u32 s36, s36, 53248\n\t"
      "v_lshl_add_u32 v89, v223, 4, s48\n\t"
      "s_movk_i32 s37, 288\n\t"
      "v_lshrrev_b32 v98, 16, v96\n\t"
      "v_lshrrev_b32 v100, 13, v99\n\t"
      "v_mad_u32_u24 v98, v98, s17, v97\n\t"
      "v_bitop3_b32 v98, v100, 32, v98 bitop3:0x6a\n\t"
      "s_waitcnt lgkmcnt(0)\n\t"
      "global_load_lds_dwordx4 v98, s[20:21] offset:-4096 nt\n\t"
      "s_mov_b64 exec, 3\n\t"
      "global_load_dword v239, v235, s[26:27]\n\t"
      "global_load_dword v239, v235, s[30:31]\n\t"
      "global_load_dword v239, v225, s[28:29]\n\t"
      "global_load_dword v239, v225, s[28:29] offset:256\n\t"
      "s_mov_b64 exec, -1\n\t"
      "global_load_dword v236, v229, s[24:25] sc1\n\t"
      "v_add_u32 v96, 49984, v96\n\t"
      "v_add_u32 v99, 349568, v99\n\t"
      "s_mov_b32 m0, s36\n\t"
      "v_lshrrev_b32 v90, 16, v88\n\t"
      "v_mad_u32_u24 v90, v90, s37, v89\n\t"
      "global_load_lds_dwordx4 v90, s[28:29] offset:-4096\n\t"
      "v_add_u32 v88, 699072, v88\n\t"
      "s_mov_b32 m0, s16\n\t"
      "v_lshrrev_b32 v98, 16, v96\n\t"
      "v_lshrrev_b32 v100, 13, v99\n\t"
      "v_mad_u32_u24 v98, v98, s17, v97\n\t"
      "v_bitop3_b32 v98, v100, 32, v98 bitop3:0x6a\n\t"
      "global_load_lds_dwordx4 v98, s[20:21] offset:-3072 nt\n\t"
      "v_add_u32 v96, 49984, v96\n\t"
      "v_add_u32 v99, 349568, v99\n\t"
      "s_mov_b32 m0, s36\n\t"
      "v_lshrrev_b32 v90, 16, v88\n\t"
      "v_mad_u32_u24 v90, v90, s37, v89\n\t"
      "global_load_lds_dwordx4 v90, s[28:29] offset:-3072\n\t"
      "v_add_u32 v88, 699072, v88\n\t"
      "s_mov_b32 m0, s16\n\t"
      "v_lshrrev_b32 v98, 16, v96\n\t"
      "v_lshrrev_b32 v100, 13, v99\n\t"
      "v_mad_u32_u24 v98, v98, s17, v97\n\t"
      "v_bitop3_b32 v98, v100, 32, v98 bitop3:0x6a\n\t"
      "global_load_lds_dwordx4 v98, s[20:21] offset:-2048 nt\n\t"
      "v_add_u32 v96, 49984, v96\n\t"
      "v_add_u32 v99, 349568, v99\n\t"
      "s_mov_b32 m0, s36\n\t"
      "v_lshrrev_b32 v90, 16, v88\n\t"
      "v_mad_u32_u24 v90, v90, s37, v89\n\t"
      "global_load_lds_dwordx4 v90, s[28:29] offset:-2048\n\t"
      "v_add_u32 v88, 699072, v88\n\t"
      "s_mov_b32 m0, s16\n\t"
      "v_lshrrev_b32 v98, 16, v96\n\t"
      "v_lshrrev_b32 v100, 13, v99\n\t"
      "v_mad_u32_u24 v98, v98, s17, v97\n\t"
      "v_bitop3_b32 v98, v100, 32, v98 bitop3:0x6a\n\t"
      "global_load_lds_dwordx4 v98, s[20:21] offset:-1024 nt\n\t"
      "v_add_u32 v96, 49984, v96\n\t"
      "v_add_u32 v99, 349568, v99\n\t"
      "s_mov_b32 m0, s36\n\t"
      "v_lshrrev_b32 v90, 16, v88\n\t"
      "v_mad_u32_u24 v90, v90, s37, v89\n\t"
      "global_load_lds_dwordx4 v90, s[28:29] offset:-1024\n\t"
      "v_add_u32 v88, 699072, v88\n\t"
      "s_mov_b32 m0, s16\n\t"
      "v_lshrrev_b32 v98, 16, v96\n\t"
      "v_lshrrev_b32 v100, 13, v99\n\t"
      "v_mad_u32_u24 v98, v98, s17, v97\n\t"
      "v_bitop3_b32 v98, v100, 32, v98 bitop3:0x6a\n\t"
      "global_load_lds_dwordx4 v98, s[20:21] offset:0 nt\n\t"
      "v_add_u32 v96, 49984, v96\n\t"
      "v_add_u32 v99, 349568, v99\n\t"
      "s_mov_b32 m0, s36\n\t"
      "v_lshrrev_b32 v90, 16, v88\n\t"
      "v_mad_u32_u24 v90, v90, s37, v89\n\t"
      "global_load_lds_dwordx4 v90, s[28:29] offset:0\n\t"
      "v_add_u32 v88, 699072, v88\n\t"
      "s_mov_b32 m0, s16\n\t"
      "v_lshrrev_b32 v98, 16, v96\n\t"
      "v_lshrrev_b32 v100, 13, v99\n\t"
      "v_mad_u32_u24 v98, v98, s17, v97\n\t"
      "v_bitop3_b32 v98, v100, 32, v98 bitop3:0x6a\n\t"
      "global_load_lds_dwordx4 v98, s[20:21] offset:1024 nt\n\t"
      "v_mov_b32 v84, 0\n\t"
      "v_mov_b32 v85, 0\n\t"
      "v_mov_b32 v86, 0\n\t"
      "v_mov_b32 v87, 0\n\t"
      "s_nop 1\n\t"
      "v_mfma_f32_32x32x16_bf16 v[68:83], v[84:87], v[84:87], 0\n\t"
      "v_and_b32 v230, 15, v223\n\t"
      "v_lshrrev_b32 v231, 4, v223\n\t"
      "s_lshr_b32 s46, s45, 1\n\t"
      "s_and_b32 s47, s45, 1\n\t"
      "s_mov_b32 s50, 0x000f000f\n\t"
      "s_mov_b32 s51, 0x000f000f\n\t"
      "s_mov_b32 s34, 114688\n\t"
      "s_mul_i32 s48, s46, 96\n\t"
      "s_mul_i32 s49, s47, 48\n\t"
      "s_sub_u32 s57, s48, s49\n\t"
      "s_add_u32 s57, s57, 48\n\t"
      "s_add_u32 s48, s48, s49\n\t"
      "v_and_b32 v226, 1, v230\n\t"
      "v_lshlrev_b32 v226, 7, v226\n\t"
      "v_lshl_add_u32 v229, v231, 4, s55\n\t"
      "v_sub_u32 v229, v229, v226\n\t"
      "v_lshlrev_b32 v232, 4, v231\n\t"
      "s_mov_b32 s49, 37632\n\t"
      "v_mfma_f32_32x32x16_bf16 v[52:67], v[84:87], v[84:87], 0\n\t"
      "v_add_u32 v225, s57, v230\n\t"
      "s_add_u32 s57, s57, 16\n\t"
      "v_mul_u32_u24 v226, 2341, v225\n\t"
      "v_lshrrev_b32 v226, 16, v226\n\t"
      "v_mul_u32_u24 v227, 28, v226\n\t"
      "v_sub_u32 v225, v225, v227\n\t"
      "v_add_u32 v96, 49984, v96\n\t"
      "v_add_u32 v99, 349568, v99\n\t"
      "s_mov_b32 m0, s36\n\t"
      "v_lshrrev_b32 v90, 16, v88\n\t"
      "v_mad_u32_u24 v90, v90, s37, v89\n\t"
      "global_load_lds_dwordx4 v90, s[28:29] offset:1024\n\t"
      "v_add_u32 v88, 699072, v88\n\t"
      "v_mad_u32_u24 v226, v226, 7, v225\n\t"
      "v_mul_u32_u24 v225, 9363, v225\n\t"
      "v_lshrrev_b32 v225, 16, v225\n\t"
      "v_mad_u32_u24 v226, v225, 42, v226\n\t"
      "v_lshlrev_b32 v225, 3, v226\n\t"
      "v_bitop3_b32 v225, v225, 32, v232 bitop3:0x6a\n\t"
      "v_mul_u32_u24 v226, 192, v226\n\t"
      "v_add3_u32 v225, v225, v226, s34\n\t"
      "v_accvgpr_write_b32 a180, v225\n\t"
      "v_add_u32 v225, s57, v230\n\t"
      "s_add_u32 s57, s57, 16\n\t"
      "v_mul_u32_u24 v226, 2341, v225\n\t"
      "v_mfma_f32_32x32x16_bf16 v[36:51], v[84:87], v[84:87], 0\n\t"
      "v_lshrrev_b32 v226, 16, v226\n\t"
      "v_mul_u32_u24 v227, 28, v226\n\t"
      "v_sub_u32 v225, v225, v227\n\t"
      "v_mad_u32_u24 v226, v226, 7, v225\n\t"
      "v_mul_u32_u24 v225, 9363, v225\n\t"
      "v_lshrrev_b32 v225, 16, v225\n\t"
      "v_mad_u32_u24 v226, v225, 42, v226\n\t"
      "v_lshlrev_b32 v225, 3, v226\n\t"
      "v_bitop3_b32 v225, v225, 32, v232 bitop3:0x6a\n\t"
      "v_mul_u32_u24 v226, 192, v226\n\t"
      "v_add3_u32 v225, v225, v226, s34\n\t"
      "v_accvgpr_write_b32 a181, v225\n\t"
      "v_add_u32 v225, s57, v230\n\t"
      "s_add_u32 s57, s57, 16\n\t"
      "v_mul_u32_u24 v226, 2341, v225\n\t"
      "v_lshrrev_b32 v226, 16, v226\n\t"
      "v_mul_u32_u24 v227, 28, v226\n\t"
      "s_mov_b32 m0, s16\n\t"
      "v_lshrrev_b32 v98, 16, v96\n\t"
      "v_lshrrev_b32 v100, 13, v99\n\t"
      "v_mad_u32_u24 v98, v98, s17, v97\n\t"
      "v_bitop3_b32 v98, v100, 32, v98 bitop3:0x6a\n\t"
      "global_load_lds_dwordx4 v98, s[20:21] offset:2048 nt\n\t"
      "v_sub_u32 v225, v225, v227\n\t"
      "v_mfma_f32_32x32x16_bf16 v[20:35], v[84:87], v[84:87], 0\n\t"
      "v_mad_u32_u24 v226, v226, 7, v225\n\t"
      "v_mul_u32_u24 v225, 9363, v225\n\t"
      "v_lshrrev_b32 v225, 16, v225\n\t"
      "v_mad_u32_u24 v226, v225, 42, v226\n\t"
      "v_lshlrev_b32 v225, 3, v226\n\t"
      "v_bitop3_b32 v225, v225, 32, v232 bitop3:0x6a\n\t"
      "v_mul_u32_u24 v226, 192, v226\n\t"
      "v_add3_u32 v225, v225, v226, s34\n\t"
      "v_accvgpr_write_b32 a182, v225\n\t"
      "v_add_u32 v225, s48, v230\n\t"
      "s_add_u32 s48, s48, 16\n\t"
      "v_mul_u32_u24 v226, 2341, v225\n\t"
      "v_lshrrev_b32 v226, 16, v226\n\t"
      "v_mul_u32_u24 v227, 192, v225\n\t"
      "v_mad_u32_u24 v227, v226, s49, v227\n\t"
      "v_add_u32 %1, v227, v229\n\t"
      "v_mul_u32_u24 v227, 28, v226\n\t"
      "v_sub_u32 v225, v225, v227\n\t"
      "v_mfma_f32_32x32x16_bf16 v[4:19], v[84:87], v[84:87], 0\n\t"
      "v_mad_u32_u24 v226, v226, 7, v225\n\t"
      "v_mul_u32_u24 v225, 9363, v225\n\t"
      "v_lshrrev_b32 v225, 16, v225\n\t"
      "v_mad_u32_u24 v226, v225, 42, v226\n\t"
      "v_lshlrev_b32 v225, 3, v226\n\t"
      "v_bitop3_b32 v225, v225, 32, v232 bitop3:0x6a\n\t"
      "v_mul_u32_u24 v226, 192, v226\n\t"
      "v_add3_u32 v225, v225, v226, s34\n\t"
      "v_accvgpr_write_b32 a183, v225\n\t"
      "v_add_u32 v225, s48, v230\n\t"
      "v_add_u32 v96, 49984, v96\n\t"
      "v_add_u32 v99, 349568, v99\n\t"
      "s_mov_b32 m0, s36\n\t"
      "v_lshrrev_b32 v90, 16, v88\n\t"
      "v_mad_u32_u24 v90, v90, s37, v89\n\t"
      "global_load_lds_dwordx4 v90, s[28:29] offset:2048\n\t"
      "v_add_u32 v88, 699072, v88\n\t"
      "s_add_u32 s48, s48, 16\n\t"
      "v_mul_u32_u24 v226, 2341, v225\n\t"
      "v_lshrrev_b32 v226, 16, v226\n\t"
      "v_mul_u32_u24 v227, 192, v225\n\t"
      "v_mad_u32_u24 v227, v226, s49, v227\n\t"
      "v_add_u32 %2, v227, v229\n\t"
      "v_mul_u32_u24 v227, 28, v226\n\t"
      "v_sub_u32 v225, v225, v227\n\t"
      "v_mfma_f32_32x32x16_bf16 a[224:239], v[84:87], v[84:87], 0\n\t"
      "v_mad_u32_u24 v226, v226, 7, v225\n\t"
      "v_mul_u32_u24 v225, 9363, v225\n\t"
      "v_lshrrev_b32 v225, 16, v225\n\t"
      "v_mad_u32_u24 v226, v225, 42, v226\n\t"
      "v_lshlrev_b32 v225, 3, v226\n\t"
      "v_bitop3_b32 v225, v225, 32, v232 bitop3:0x6a\n\t"
      "v_mul_u32_u24 v226, 192, v226\n\t"
      "v_add3_u32 v225, v225, v226, s34\n\t"
      "v_accvgpr_write_b32 a184, v225\n\t"
      "v_add_u32 v225, s48, v230\n\t"
      "s_add_u32 s48, s48, 16\n\t"
      "v_mul_u32_u24 v226, 2341, v225\n\t"
      "v_lshrrev_b32 v226, 16, v226\n\t"
      "v_mul_u32_u24 v227, 192, v225\n\t"
      "v_mad_u32_u24 v227, v226, s49, v227\n\t"
      "v_add_u32 %3, v227, v229\n\t"
      "v_mul_u32_u24 v227, 28, v226\n\t"
      "v_sub_u32 v225, v225, v227\n\t"
      "v_mfma_f32_32x32x16_bf16 a[240:255], v[84:87], v[84:87], 0\n\t"
      "v_mad_u32_u24 v226, v226, 7, v225\n\t"
      "v_mul_u32_u24 v225, 9363, v225\n\t"
      "s_mov_b32 m0, s16\n\t"
      "v_lshrrev_b32 v98, 16, v96\n\t"
      "v_lshrrev_b32 v100, 13, v99\n\t"
      "v_mad_u32_u24 v98, v98, s17, v97\n\t"
      "v_bitop3_b32 v98, v100, 32, v98 bitop3:0x6a\n\t"
      "global_load_lds_dwordx4 v98, s[20:21] offset:3072 nt\n\t"
      "v_lshrrev_b32 v225, 16, v225\n\t"
      "v_mad_u32_u24 v226, v225, 42, v226\n\t"
      "v_lshlrev_b32 v225, 3, v226\n\t"
      "v_bitop3_b32 v225, v225, 32, v232 bitop3:0x6a\n\t"
      "v_mul_u32_u24 v226, 192, v226\n\t"
      "v_add3_u32 v225, v225, v226, s34\n\t"
      "v_accvgpr_write_b32 a185, v225\n\t"
      "v_lshrrev_b32 v225, 2, v230\n\t"
      "v_and_b32 v226, 3, v230\n\t"
      "v_mad_u32_u24 v225, v225, 28, v226\n\t"
      "v_lshrrev_b32 v226, 3, v230\n\t"
      "v_mul_u32_u24 v226, 1512, v226\n\t"
      "v_add_u32 v225, v225, v226\n\t"
      "v_mul_u32_u24 v225, 192, v225\n\t"
      "v_lshl_add_u32 v226, v231, 4, s33\n\t"
      "v_add_u32 %4, v225, v226\n\t"
      "v_mfma_f32_32x32x16_bf16 a[0:15], v[84:87], v[84:87], 0\n\t"
      "v_mul_u32_u24 v238, 192, v230\n\t"
      "v_lshl_add_u32 v238, v231, 4, v238\n\t"
      "v_add_u32 v238, 155392, v238\n\t"
      "v_mov_b32 v208, 0x3f803f80\n\t"
      "v_mov_b32 v209, 0x3f803f80\n\t"
      "v_mov_b32 v210, 0x3f803f80\n\t"
      "v_mov_b32 v211, 0x3f803f80\n\t"
      "v_mov_b32 v212, 0x3f803f80\n\t"
      "v_mov_b32 v213, 0x3f803f80\n\t"
      "v_cmp_eq_u32 vcc, 0, v231\n\t"
      "v_mov_b32 v225, 0x3f80\n\t"
      "v_mov_b32 v215, 0\n\t"
      "v_cndmask_b32 v214, 0, v225, vcc\n\t"
      "v_add_u32 v96, 49984, v96\n\t"
      "v_add_u32 v99, 349568, v99\n\t"
      "s_mov_b32 m0, s36\n\t"
      "v_lshrrev_b32 v90, 16, v88\n\t"
      "v_mad_u32_u24 v90, v90, s37, v89\n\t"
      "global_load_lds_dwordx4 v90, s[28:29] offset:3072\n\t"
      "v_add_u32 v88, 699072, v88\n\t"
      "s_lshl_b32 s48, s47, 1\n\t"
      "s_add_u32 s49, s48, 1\n\t"
      "s_add_u32 s48, s48, s46\n\t"
      "s_sub_u32 s49, s49, s46\n\t"
      "v_bfe_u32 v225, v223, 3, 1\n\t"
      "v_mfma_f32_32x32x16_bf16 a[16:31], v[84:87], v[84:87], 0\n\t"
      "v_and_b32 v226, 7, v223\n\t"
      "s_lshl_b32 s57, s48, 11\n\t"
      "s_add_u32 s57, s57, 49152\n\t"
      "v_lshl_add_u32 v221, v226, 4, s57\n\t"
      "s_add_u32 s57, s57, 1024\n\t"
      "v_lshl_add_u32 v235, v226, 7, s57\n\t"
      "v_mul_u32_u24 v225, 57344, v225\n\t"
      "v_add_u32 v224, v225, v221\n\t"
      "s_mov_b64 s[58:59], s[24:25]\n\t"
      "s_mul_i32 s48, s48, 12288\n\t"
      "s_mul_i32 s49, s49, 12288\n\t"
      "v_lshlrev_b32 v217, 4, v223\n\t"
      "v_add_u32 v218, s49, v217\n\t"
      "v_add_u32 v217, s48, v217\n\t"
      "v_add_u32 v219, 3072, v217\n\t"
      "v_add_u32 v220, 9216, v217\n\t"
      "s_add_u32 s42, s48, 3072\n\t"
      "s_add_u32 s43, s48, 9216\n\t"
      "v_mfma_f32_32x32x16_bf16 a[32:47], v[84:87], v[84:87], 0\n\t"
      "s_mov_b32 s44, 5\n\t"
      "v_and_b32 v228, 3, v230\n\t"
      "v_lshrrev_b32 v225, 2, v223\n\t"
      "s_mul_i32 s48, s45, 9216\n\t"
      "s_add_u32 s48, s48, 49152\n\t"
      "v_lshl_add_u32 v226, v228, 3, s48\n\t"
      "s_add_u32 s16, s16, 8192\n\t"
      "v_add_u32 v97, 8192, v97\n\t"
      "s_mov_b32 m0, s16\n\t"
      "v_lshrrev_b32 v98, 16, v96\n\t"
      "v_lshrrev_b32 v100, 13, v99\n\t"
      "v_mad_u32_u24 v98, v98, s17, v97\n\t"
      "v_bitop3_b32 v98, v100, 32, v98 bitop3:0x6a\n\t"
      "global_load_lds_dwordx4 v98, s[20:21] offset:-4096 nt\n\t"
      "v_mul_u32_u24 v234, 96, v225\n\t"
      "v_add_u32 v234, v234, v226\n\t"
      "s_mul_i32 s48, s45, 49\n\t"
      "v_add_u32 v225, s48, v225\n\t"
      "v_mul_u32_u24 v232, 192, v225\n\t"
      "v_lshlrev_b32 v225, 3, v225\n\t"
      "v_and_b32 v225, 32, v225\n\t"
      "v_lshl_add_u32 v232, v228, 3, v232\n\t"
      "v_add_u32 v232, 114688, v232\n\t"
      "v_sub_u32 v227, v232, v225\n\t"
      "v_add_u32 v232, v232, v225\n\t"
      "s_add_u32 s48, s48, 48\n\t"
      "v_mfma_f32_32x32x16_bf16 a[48:63], v[84:87], v[84:87], 0\n\t"
      "s_lshl_b32 s49, s48, 3\n\t"
      "s_and_b32 s49, s49, 32\n\t"
      "s_mul_i32 s48, s48, 192\n\t"
      "s_add_u32 s48, s48, 114688\n\t"
      "v_lshl_add_u32 v233, v228, 3, s48\n\t"
      "v_subrev_u32 v226, s49, v233\n\t"
      "v_add_u32 v233, s49, v233\n\t"
      "s_mul_i32 s48, s45, 192\n\t"
      "s_add_u32 s48, s48, 152320\n\t"
      "v_lshrrev_b32 v229, 2, v230\n\t"
      "v_lshl_add_u32 v229, v229, 4, s48\n\t"
      "v_lshl_add_u32 v229, v228, 1, v229\n\t"
      "v_mul_u32_u24 v237, 192, v228\n\t"
      "v_lshl_add_u32 v237, v231, 4, v237\n\t"
      "v_add_u32 v237, 152320, v237\n\t"
      "s_and_b32 s19, %6, 7\n\t"
      "s_lshl_b32 s19, s19, 5\n\t"
      "v_add_u32 v96, 49984, v96\n\t"
      "v_add_u32 v99, 349568, v99\n\t"
      "s_mov_b32 m0, s36\n\t"
      "s_add_u32 m0, s36, 8192\n\t"
      "v_add_u32 v89, 8192, v89\n\t"
      "v_lshrrev_b32 v90, 16, v88\n\t"
      "v_mad_u32_u24 v90, v90, s37, v89\n\t"
      "global_load_lds_dwordx4 v90, s[28:29] offset:-4096\n\t"
      "v_add_u32 v88, 699072, v88\n\t"
      "s_lshr_b32 s48, %6, 3\n\t"
      "v_mfma_f32_32x32x16_bf16 a[64:79], v[84:87], v[84:87], 0\n\t"
      "s_add_u32 s19, s19, s48\n\t"
      "v_lshlrev_b32 v243, 4, v223\n\t"
      "s_mul_i32 s48, s45, 18432\n\t"
      "s_cmp_eq_u32 s45, 3\n\t"
      "s_cselect_b32 s48, 114688, s48\n\t"
      "s_cselect_b32 s57, 0, s45\n\t"
      "s_cselect_b64 s[38:39], 0, -1\n\t"
      "v_add_u32 v240, s48, v243\n\t"
      "s_xor_b32 s49, s45, 1\n\t"
      "s_mul_i32 s48, s49, 18432\n\t"
      "s_cmp_eq_u32 s49, 3\n\t"
      "s_cselect_b32 s48, 114688, s48\n\t"
      "v_add_u32 v241, s48, v243\n\t"
      "s_mul_i32 s48, s45, 6144\n\t"
      "s_add_u32 s48, s48, 133120\n\t"
      "v_add_u32 v242, s48, v243\n\t"
      "v_lshlrev_b32 v247, 2, v230\n\t"
      "s_mul_i32 s49, s49, 448\n\t"
      "s_add_u32 s49, s49, 55296\n\t"
      "v_add_u32 v245, s49, v247\n\t"
      "s_mul_i32 s48, s45, 448\n\t"
      "s_add_u32 s48, s48, 55296\n\t"
      "v_mfma_f32_32x32x16_bf16 a[80:95], v[84:87], v[84:87], 0\n\t"
      "v_cmp_gt_u32 vcc, 16, v223\n\t"
      "v_add_u32 v244, s48, v247\n\t"
      "v_mov_b32 v246, 57344\n\t"
      "v_cndmask_b32 v244, v246, v244, vcc\n\t"
      "v_add_u32 v246, 55680, v247\n\t"
      "s_lshl_b32 s48, s57, 11\n\t"
      "s_add_u32 s48, s48, 133120\n\t"
      "v_add_u32 v243, s48, v243\n\t"
      "s_lshl_b32 s48, s57, 6\n\t"
      "v_add_u32 v248, s48, %4\n\t"
      "s_mov_b32 m0, s16\n\t"
      "v_lshrrev_b32 v98, 16, v96\n\t"
      "v_lshrrev_b32 v100, 13, v99\n\t"
      "v_mad_u32_u24 v98, v98, s17, v97\n\t"
      "v_bitop3_b32 v98, v100, 32, v98 bitop3:0x6a\n\t"
      "s_mov_b64 exec, 0xfff\n\t"
      "global_load_lds_dwordx4 v98, s[20:21] offset:-3072 nt\n\t"
      "s_mov_b64 exec, -1\n\t"
      "s_mov_b64 s[40:41], s[24:25]\n\t"
      "s_cmp_eq_u32 s45, 3\n\t"
      "s_cselect_b32 s57, 0, s45\n\t"
      "v_lshrrev_b32 v216, 2, v230\n\t"
      "v_lshl_add_u32 v216, v216, 3, v228\n\t"
      "s_mul_i32 s48, s57, 6144\n\t"
      "v_mul_u32_u24 v216, 192, v216\n\t"
      "v_lshl_add_u32 v222, v231, 4, s48\n\t"
      "v_add_u32 v216, v216, v222\n\t"
      "v_lshrrev_b32 v222, 2, v223\n\t"
      "v_mul_u32_u24 v222, 192, v222\n\t"
      "s_lshl_b32 s48, s57, 6\n\t"
      "s_add_u32 s48, s48, 2048\n\t"
      "v_lshl_add_u32 v225, v228, 4, s48\n\t"
      "s_mul_i32 s49, s45, 6144\n\t"
      "s_add_u32 s49, s49, 88064\n\t"
      "s_mov_b32 m0, s49\n\t"
      "v_add_u32 v222, v222, v225\n\t"
      "s_cmp_eq_u32 s45, 3\n\t"
      "s_cbranch_scc1 Lwl1%=\n\t"
      "global_load_lds_dwordx4 v222, s[30:31] offset:-2048\n\t"
      "v_add_u32 v222, 2048, v222\n\t"
      "global_load_lds_dwordx4 v222, s[30:31] offset:-1024\n\t"
      "v_add_u32 v222, 2048, v222\n\t"
      "global_load_lds_dwordx4 v222, s[30:31] offset:0\n\t"
      "v_add_u32 v222, 2048, v222\n\t"
      "global_load_lds_dwordx4 v222, s[30:31] offset:1024\n\t"
      "v_add_u32 v222, 2048, v222\n\t"
      "global_load_lds_dwordx4 v222, s[30:31] offset:2048\n\t"
      "v_add_u32 v222, 2048, v222\n\t"
      "global_load_lds_dwordx4 v222, s[30:31] offset:3072\n\t"
      "Lwl1%=:\n\t"
      "s_cmp_lg_u32 s14, 0\n\t"
      "s_cbranch_scc1 Lno6%=\n\t"
      "s_mov_b32 m0, s15\n\t"
      "s_nop 0\n\t"
      "global_load_lds_dwordx4 v92, s[20:21] nt\n\t"
      "Lno6%=:\n\t"
      "s_cmp_eq_u32 s45, 3\n\t"
      "s_waitcnt vmcnt(14)\n\t"
      "s_cbranch_scc0 Lwl2%=\n\t"
      "s_waitcnt vmcnt(8)\n\t"
      "Lwl2%=:\n\t"
      "ds_read_b64_tr_b16 v[76:77], v232 offset:0\n\t"
      "ds_read_b64_tr_b16 v[78:79], v232 offset:3072\n\t"
      "ds_read_b64_tr_b16 v[84:85], v227 offset:32\n\t"
      "ds_read_b64_tr_b16 v[86:87], v227 offset:3104\n\t"
      "ds_read_b64_tr_b16 v[92:93], v232 offset:64\n\t"
      "ds_read_b64_tr_b16 v[94:95], v232 offset:3136\n\t"
      "ds_read_b64_tr_b16 v[100:101], v227 offset:96\n\t"
      "ds_read_b64_tr_b16 v[102:103], v227 offset:3168\n\t"
      "ds_read_b64_tr_b16 v[108:109], v232 offset:128\n\t"
      "ds_read_b64_tr_b16 v[110:111], v232 offset:3200\n\t"
      "ds_read_b64_tr_b16 v[116:117], v227 offset:160\n\t"
      "ds_read_b64_tr_b16 v[118:119], v227 offset:3232\n\t"
      "s_waitcnt lgkmcnt(8)\n\t"
      "v_mfma_f32_16x16x32_bf16 v[124:127], v[208:211], v[76:79], 0\n\t"
      "v_mfma_f32_16x16x32_bf16 v[128:131], v[208:211], v[84:87], 0\n\t"
      "s_waitcnt lgkmcnt(4)\n\t"
      "v_mfma_f32_16x16x32_bf16 v[132:135], v[208:211], v[92:95], 0\n\t"
      "v_mfma_f32_16x16x32_bf16 v[136:139], v[208:211], v[100:103], 0\n\t"
      "s_waitcnt lgkmcnt(0)\n\t"
      "v_mfma_f32_16x16x32_bf16 v[140:143], v[208:211], v[108:111], 0\n\t"
      "v_mfma_f32_16x16x32_bf16 v[144:147], v[208:211], v[116:119], 0\n\t"
      "s_waitcnt vmcnt(7)\n\t"
      "s_cbranch_scc0 Lwl3%=\n\t"
      "s_waitcnt vmcnt(1)\n\t"
      "Lwl3%=:\n\t"
      "ds_read_b64_tr_b16 v[148:149], v234 offset:0\n\t"
      "ds_read_b64_tr_b16 v[150:151], v234 offset:1536\n\t"
      "ds_read_b64_tr_b16 v[160:161], v234 offset:32\n\t"
      "ds_read_b64_tr_b16 v[162:163], v234 offset:1568\n\t"
      "ds_read_b64_tr_b16 v[172:173], v234 offset:64\n\t"
      "ds_read_b64_tr_b16 v[174:175], v234 offset:1600\n\t"
      "ds_read_b64_tr_b16 v[152:153], v234 offset:3072\n\t"
      "ds_read_b64_tr_b16 v[154:155], v234 offset:4608\n\t"
      "ds_read_b64_tr_b16 v[164:165], v234 offset:3104\n\t"
      "ds_read_b64_tr_b16 v[166:167], v234 offset:4640\n\t"
      "ds_read_b64_tr_b16 v[176:177], v234 offset:3136\n\t"
      "ds_read_b64_tr_b16 v[178:179], v234 offset:4672\n\t"
      "ds_read_b64_tr_b16 v[156:157], v234 offset:6144\n\t"
      "ds_read_b64_tr_b16 v[158:159], v234 offset:7680\n\t"
      "ds_read_b64_tr_b16 v[168:169], v234 offset:6176\n\t"
      "ds_read_b64_tr_b16 v[170:171], v234 offset:7712\n\t"
      "ds_read_b64_tr_b16 v[180:181], v234 offset:6208\n\t"
      "ds_read_b64_tr_b16 v[182:183], v234 offset:7744\n\t"
      "s_mov_b32 s48, 0x3ca72f05\n\t"
      "s_waitcnt vmcnt(6)\n\t"
      "s_cbranch_scc0 Lwl4%=\n\t"
      "s_waitcnt vmcnt(0)\n\t"
      "Lwl4%=:\n\t"
      "ds_read_b64_tr_b16 v[80:81], v232 offset:6144\n\t"
      "ds_read_b64_tr_b16 v[82:83], v233 offset:0\n\t"
      "ds_read_b64_tr_b16 v[88:89], v227 offset:6176\n\t"
      "ds_read_b64_tr_b16 v[90:91], v226 offset:32\n\t"
      "ds_read_b64_tr_b16 v[96:97], v232 offset:6208\n\t"
      "ds_read_b64_tr_b16 v[98:99], v233 offset:64\n\t"
      "ds_read_b64_tr_b16 v[104:105], v227 offset:6240\n\t"
      "ds_read_b64_tr_b16 v[106:107], v226 offset:96\n\t"
      "ds_read_b64_tr_b16 v[112:113], v232 offset:6272\n\t"
      "ds_read_b64_tr_b16 v[114:115], v233 offset:128\n\t"
      "ds_read_b64_tr_b16 v[120:121], v227 offset:6304\n\t"
      "ds_read_b64_tr_b16 v[122:123], v226 offset:160\n\t"
      "s_cbranch_scc1 Lwl5%=\n\t"
      "global_load_dwordx4 v[184:187], v216, s[26:27] offset:0\n\t"
      "global_load_dwordx4 v[188:191], v216, s[26:27] offset:64\n\t"
      "Lwl5%=:\n\t"
      "s_waitcnt lgkmcnt(8)\n\t"
      "v_mfma_f32_16x16x32_bf16 v[124:127], v[212:215], v[80:83], v[124:127]\n\t"
      "v_mfma_f32_16x16x32_bf16 v[128:131], v[212:215], v[88:91], v[128:131]\n\t"
      "s_waitcnt lgkmcnt(4)\n\t"
      "v_mfma_f32_16x16x32_bf16 v[132:135], v[212:215], v[96:99], v[132:135]\n\t"
      "v_mfma_f32_16x16x32_bf16 v[136:139], v[212:215], v[104:107], v[136:139]\n\t"
      "s_waitcnt lgkmcnt(0)\n\t"
      "v_mfma_f32_16x16x32_bf16 v[140:143], v[212:215], v[112:115], v[140:143]\n\t"
      "v_mfma_f32_16x16x32_bf16 v[144:147], v[212:215], v[120:123], v[144:147]\n\t"
      "s_cbranch_scc1 Lwl6%=\n\t"
      "global_load_dwordx4 v[192:195], v216, s[26:27] offset:128\n\t"
      "global_load_dwordx4 v[196:199], v216, s[26:27] offset:768\n\t"
      "Lwl6%=:\n\t"
      "v_mul_f32 v124, s48, v124\n\t"
      "v_mul_f32 v128, s48, v128\n\t"
      "v_cvt_pk_bf16_f32 v76, v124, v128\n\t"
      "v_mul_f32 v132, s48, v132\n\t"
      "v_mul_f32 v136, s48, v136\n\t"
      "v_cvt_pk_bf16_f32 v77, v132, v136\n\t"
      "v_mul_f32 v140, s48, v140\n\t"
      "v_mul_f32 v144, s48, v144\n\t"
      "v_cvt_pk_bf16_f32 v78, v140, v144\n\t"
      "s_mov_b64 exec, 0xffff\n\t"
      "ds_write_b16 v229, v76 offset:0\n\t"
      "ds_write_b16_d16_hi v229, v76 offset:8\n\t"
      "ds_write_b16 v229, v77 offset:64\n\t"
      "ds_write_b16_d16_hi v229, v77 offset:72\n\t"
      "ds_write_b16 v229, v78 offset:128\n\t"
      "ds_write_b16_d16_hi v229, v78 offset:136\n\t"
      "s_mov_b64 exec, -1\n\t"
      "s_cbranch_scc1 Lwl7%=\n\t"
      "global_load_dwordx4 v[200:203], v216, s[26:27] offset:832\n\t"
      "global_load_dwordx4 v[204:207], v216, s[26:27] offset:896\n\t"
      "Lwl7%=:\n\t"
      "s_waitcnt lgkmcnt(0)\n\t"
      "s_barrier\n\t"
      "ds_read_b128 v[124:127], v237 offset:0\n\t"
      "ds_read_b128 v[128:131], v237 offset:64\n\t"
      "ds_read_b128 v[132:135], v237 offset:128\n\t"
      "v_add_u32 v236, 0x9e3779b9, v236\n\t"
      "v_mul_u32_u24 v233, 384, v228\n\t"
      "s_mul_i32 s48, s45, 96\n\t"
      "s_add_u32 s48, s48, 153088\n\t"
      "v_readfirstlane_b32 s54, v236\n\t"
      "v_lshl_add_u32 v229, v231, 3, s48\n\t"
      "v_add_u32 v229, v229, v233\n\t"
      "v_lshl_add_u32 v233, v231, 4, v233\n\t"
      "v_add_u32 v233, 153088, v233\n\t"
      "v_lshrrev_b32 v225, 2, v230\n\t"
      "v_lshl_add_u32 v225, v231, 3, v225\n\t"
      "s_mul_i32 s48, s45, 6144\n\t"
      "s_add_u32 s48, s48, 86016\n\t"
      "v_lshl_add_u32 v226, v228, 4, s48\n\t"
      "v_lshl_add_u32 v234, v225, 6, v226\n\t"
      "s_waitcnt vmcnt(6)\n\t"
      "s_waitcnt lgkmcnt(2)\n\t"
      "v_mfma_f32_16x16x32_bf16 v[136:139], v[148:151], v[124:127], 0\n\t"
      "ds_read_b64_tr_b16 v[76:77], v234 offset:0\n\t"
      "v_mfma_f32_16x16x32_bf16 v[140:143], v[160:163], v[124:127], 0\n\t"
      "ds_read_b64_tr_b16 v[78:79], v234 offset:256\n\t"
      "v_mfma_f32_16x16x32_bf16 v[144:147], v[172:175], v[124:127], 0\n\t"
      "ds_read_b64_tr_b16 v[88:89], v234 offset:8\n\t"
      "s_waitcnt lgkmcnt(4)\n\t"
      "v_mfma_f32_16x16x32_bf16 v[136:139], v[152:155], v[128:131], v[136:139]\n\t"
      "ds_read_b64_tr_b16 v[90:91], v234 offset:264\n\t"
      "v_mfma_f32_16x16x32_bf16 v[140:143], v[164:167], v[128:131], v[140:143]\n\t"
      "ds_read_b64_tr_b16 v[80:81], v234 offset:2048\n\t"
      "v_mfma_f32_16x16x32_bf16 v[144:147], v[176:179], v[128:131], v[144:147]\n\t"
      "ds_read_b64_tr_b16 v[82:83], v234 offset:2304\n\t"
      "s_waitcnt lgkmcnt(6)\n\t"
      "v_mfma_f32_16x16x32_bf16 v[136:139], v[156:159], v[132:135], v[136:139]\n\t"
      "ds_read_b64_tr_b16 v[92:93], v234 offset:2056\n\t"
      "v_mfma_f32_16x16x32_bf16 v[140:143], v[168:171], v[132:135], v[140:143]\n\t"
      "ds_read_b64_tr_b16 v[94:95], v234 offset:2312\n\t"
      "v_mfma_f32_16x16x32_bf16 v[144:147], v[180:183], v[132:135], v[144:147]\n\t"
      "ds_read_b64_tr_b16 v[84:85], v234 offset:4096\n\t"
      "ds_read_b64_tr_b16 v[86:87], v234 offset:4352\n\t"
      "ds_read_b64_tr_b16 v[96:97], v234 offset:4104\n\t"
      "ds_read_b64_tr_b16 v[98:99], v234 offset:4360\n\t"
      "v_cvt_pk_bf16_f32 v124, v136, v137\n\t"
      "v_cvt_pk_bf16_f32 v125, v138, v139\n\t"
      "v_cvt_pk_bf16_f32 v126, v140, v141\n\t"
      "v_cvt_pk_bf16_f32 v127, v142, v143\n\t"
      "v_cvt_pk_bf16_f32 v128, v144, v145\n\t"
      "v_cvt_pk_bf16_f32 v129, v146, v147\n\t"
      "s_mov_b64 exec, s[50:51]\n\t"
      "ds_write_b64 v229, v[124:125] offset:0\n\t"
      "ds_write_b64 v229, v[126:127] offset:32\n\t"
      "ds_write_b64 v229, v[128:129] offset:64\n\t"
      "s_mov_b64 exec, -1\n\t"
      "s_cmp_eq_u32 s45, 3\n\t"
      "s_waitcnt vmcnt(0) lgkmcnt(0)\n\t"
      "s_barrier\n\t"
      "s_cbranch_scc1 Lw3d%=\n\t"
      "ds_read_b128 v[100:103], v233 offset:0\n\t"
      "ds_read_b128 v[112:115], v233 offset:192\n\t"
      "ds_read_b128 v[104:107], v233 offset:64\n\t"
      "ds_read_b128 v[116:119], v233 offset:256\n\t"
      "ds_read_b128 v[108:111], v233 offset:128\n\t"
      "ds_read_b128 v[120:123], v233 offset:320\n\t"
      "s_lshr_b32 s56, s19, 5\n\t"
      "s_mul_i32 s56, s56, 57344\n\t"
      "s_bfe_u32 s48, s19, 0x20003\n\t"
      "s_mul_i32 s48, s48, 12288\n\t"
      "s_add_u32 s56, s56, s48\n\t"
      "s_bfe_u32 s48, s19, 0x10002\n\t"
      "s_and_b32 s49, s19, 3\n\t"
      "s_mul_i32 s57, s48, 3072\n\t"
      "s_add_u32 s57, s57, s56\n\t"
      "s_lshl_b32 s52, s45, 10\n\t"
      "s_add_u32 s57, s57, s52\n\t"
      "s_lshl_b32 s52, s49, 6\n\t"
      "s_add_u32 s57, s57, s52\n\t"
      "v_lshl_add_u32 v225, v223, 4, s57\n\t"
      "s_lshl_b32 s48, s48, 3\n\t"
      "s_lshl_b32 s49, s49, 8\n\t"
      "s_add_u32 s48, s48, s49\n\t"
      "s_lshl_b32 s49, s45, 11\n\t"
      "s_add_u32 s48, s48, s49\n\t"
      "s_add_u32 s48, s48, s56\n\t"
      "s_add_u32 s48, s48, 6144\n\t"
      "v_lshl_add_u32 v226, v230, 4, s48\n\t"
      "v_lshl_add_u32 v226, v231, 10, v226\n\t"
      "s_lshl_b32 s48, s45, 8\n\t"
      "s_add_u32 s48, s48, 154624\n\t"
      "v_lshl_add_u32 v227, v228, 4, s48\n\t"
      "v_lshl_add_u32 v227, v231, 6, v227\n\t"
      "v_cmp_eq_u32 vcc, 1, v231\n\t"
      "s_mov_b32 s48, 0x3e16c740\n\t"
      "s_mov_b32 s49, 0x3e16c740\n\t"
      "s_waitcnt lgkmcnt(4)\n\t"
      "v_mfma_f32_16x16x32_bf16 v[124:127], v[184:187], v[100:103], 0\n\t"
      "v_mfma_f32_16x16x32_bf16 v[128:131], v[196:199], v[100:103], 0\n\t"
      "v_mfma_f32_16x16x32_bf16 v[132:135], v[112:115], v[76:79], 0\n\t"
      "v_mfma_f32_16x16x32_bf16 v[136:139], v[112:115], v[88:91], 0\n\t"
      "s_waitcnt lgkmcnt(2)\n\t"
      "v_mfma_f32_16x16x32_bf16 v[124:127], v[188:191], v[104:107], v[124:127]\n\t"
      "v_mfma_f32_16x16x32_bf16 v[128:131], v[200:203], v[104:107], v[128:131]\n\t"
      "v_mfma_f32_16x16x32_bf16 v[132:135], v[116:119], v[80:83], v[132:135]\n\t"
      "v_mfma_f32_16x16x32_bf16 v[136:139], v[116:119], v[92:95], v[136:139]\n\t"
      "s_waitcnt lgkmcnt(0)\n\t"
      "v_mfma_f32_16x16x32_bf16 v[124:127], v[192:195], v[108:111], v[124:127]\n\t"
      "v_mfma_f32_16x16x32_bf16 v[128:131], v[204:207], v[108:111], v[128:131]\n\t"
      "v_mfma_f32_16x16x32_bf16 v[132:135], v[120:123], v[84:87], v[132:135]\n\t"
      "v_mfma_f32_16x16x32_bf16 v[136:139], v[120:123], v[96:99], v[136:139]\n\t"
      "s_nop 4\n\t"
      "v_pk_mul_f32 v[124:125], v[124:125], s[48:49]\n\t"
      "v_pk_mul_f32 v[126:127], v[126:127], s[48:49]\n\t"
      "v_pk_mul_f32 v[128:129], v[128:129], s[48:49]\n\t"
      "v_pk_mul_f32 v[130:131], v[130:131], s[48:49]\n\t"
      "v_cvt_pk_bf16_f32 v140, v124, v125\n\t"
      "v_cvt_pk_bf16_f32 v141, v126, v127\n\t"
      "v_cvt_pk_bf16_f32 v142, v128, v129\n\t"
      "v_cvt_pk_bf16_f32 v143, v130, v131\n\t"
      "s_mov_b64 exec, s[50:51]\n\t"
      "global_store_dwordx4 v225, v[140:143], s[40:41] sc1\n\t"
      "ds_write_b128 v227, v[140:143]\n\t"
      "s_mov_b64 exec, -1\n\t"
      "v_cvt_pk_bf16_f32 v144, v132, v133\n\t"
      "v_cvt_pk_bf16_f32 v145, v134, v135\n\t"
      "v_cvt_pk_bf16_f32 v146, v136, v137\n\t"
      "v_cvt_pk_bf16_f32 v147, v138, v139\n\t"
      "v_cndmask_b32 v144, v144, v146, vcc\n\t"
      "v_cndmask_b32 v145, v145, v147, vcc\n\t"
      "s_mov_b32 exec_hi, 0\n\t"
      "global_store_dwordx2 v226, v[144:145], s[40:41] sc1\n\t"
      "s_mov_b64 exec, -1\n\t"
      "v_max3_f32 v124, |v132|, |v133|, |v134|\n\t"
      "v_max3_f32 v125, |v135|, |v136|, |v137|\n\t"
      "v_max3_f32 v124, |v138|, |v139|, v124\n\t"
      "v_max_f32 v124, v124, v125\n\t"
      "v_cmp_lt_f32 vcc, 0x4c800000, v124\n\t"
      "v_mov_b32 v125, 524544\n\t"
      "s_cbranch_vccz Lw3d%=\n\t"
      "s_mov_b64 exec, 1\n\t"
      "global_store_dword v125, v236, s[40:41] sc1\n\t"
      "s_mov_b64 exec, -1\n\t"
      "Lw3d%=:\n\t"
      "v_accvgpr_read_b32 v226, a180\n\t"
      "v_accvgpr_read_b32 v229, a181\n\t"
      "v_accvgpr_read_b32 v232, a182\n\t"
      "v_accvgpr_read_b32 v233, a183\n\t"
      "v_accvgpr_read_b32 v234, a184\n\t"
      "v_accvgpr_read_b32 v249, a185\n\t"
      "ds_read_b128 a[96:99], v226 offset:0\n\t"
      "ds_read_b128 a[100:103], v226 offset:64\n\t"
      "ds_read_b128 a[104:107], v226 offset:128\n\t"
      "ds_read_b128 a[108:111], v229 offset:0\n\t"
      "ds_read_b128 a[112:115], v229 offset:64\n\t"
      "ds_read_b128 a[116:119], v229 offset:128\n\t"
      "ds_read_b128 a[120:123], v232 offset:0\n\t"
      "ds_read_b128 a[124:127], v232 offset:64\n\t"
      "ds_read_b128 a[128:131], v232 offset:128\n\t"
      "ds_read_b128 a[132:135], v233 offset:0\n\t"
      "ds_read_b128 a[136:139], v233 offset:64\n\t"
      "ds_read_b128 a[140:143], v233 offset:128\n\t"
      "ds_read_b128 a[144:147], v234 offset:0\n\t"
      "ds_read_b128 a[148:151], v234 offset:64\n\t"
      "ds_read_b128 a[152:155], v234 offset:128\n\t"
      "ds_read_b128 a[156:159], v249 offset:0\n\t"
      "ds_read_b128 a[160:163], v249 offset:64\n\t"
      "ds_read_b128 a[164:167], v249 offset:128\n\t"
      "ds_read_b128 a[168:171], v238 offset:0\n\t"
      "ds_read_b128 a[172:175], v238 offset:64\n\t"
      "ds_read_b128 a[176:179], v238 offset:128\n\t"
      "v_lshlrev_b32 v227, 4, v228\n\t"
      "v_lshl_add_u32 v227, v231, 6, v227\n\t"
      "v_add_u32 v227, 154624, v227\n\t"
      "v_cmp_gt_u32 vcc, 2, v230\n\t"
      "v_mov_b32 v125, 0x3f803f80\n\t"
      "s_lshr_b32 s48, s19, 5\n\t"
      "s_mul_i32 s48, s48, 57344\n\t"
      "v_cndmask_b32 v212, 0, v125, vcc\n\t"
      "v_cndmask_b32 v213, 0, v125, vcc\n\t"
      "v_cndmask_b32 v214, 0, v125, vcc\n\t"
      "v_cndmask_b32 v215, 0, v125, vcc\n\t"
      "v_cmp_eq_u32 vcc, 1, v230\n\t"
      "v_mov_b32 v216, 0x44444444\n\t"
      "v_mov_b32 v125, 0xeeeeeeee\n\t"
      "v_cndmask_b32 v216, v216, v125, vcc\n\t"
      "s_bfe_u32 s49, s19, 0x20003\n\t"
      "s_lshl_b32 s49, s49, 11\n\t"
      "s_add_u32 s48, s48, s49\n\t"
      "s_and_b32 s49, s19, 7\n\t"
      "s_lshl_b32 s49, s49, 4\n\t"
      "s_add_u32 s48, s48, s49\n\t"
      "s_add_u32 s48, s48, 49152\n\t"
      "s_mul_i32 s49, s49, 7\n\t"
      "s_add_u32 s49, s49, 1024\n\t"
      "v_mul_u32_u24 v225, s49, v223\n\t"
      "v_add_u32 v225, s48, v225\n\t"
      "s_cmp_lg_u32 s45, 3\n\t"
      "s_waitcnt vmcnt(0) lgkmcnt(15)\n\t"
      "s_barrier\n\t"
      "s_cbranch_scc1 Lw3e%=\n\t"
      "s_mov_b64 exec, 3\n\t"
      "global_store_dword v225, v236, s[40:41] sc1\n\t"
      "s_mov_b64 exec, -1\n\t"
      "Lw3e%=:\n\t"
      "ds_read_b128 v[76:79], v227 offset:0\n\t"
      "ds_read_b128 v[80:83], v227 offset:256\n\t"
      "ds_read_b128 v[84:87], v227 offset:512\n\t"
      "s_mov_b32 m0, s42\n\t"
      "s_mov_b32 s57, 0\n\t"
      "s_waitcnt lgkmcnt(0)\n\t"
      "v_mfma_f32_16x16x32_bf16 v[128:131], v[76:79], a[96:99], 0\n\t"
      "v_mfma_f32_16x16x32_bf16 v[128:131], v[80:83], a[100:103], v[128:131]\n\t"
      "v_mfma_f32_16x16x32_bf16 v[128:131], v[84:87], a[104:107], v[128:131]\n\t"
      "v_mfma_f32_16x16x32_bf16 v[132:135], v[76:79], a[108:111], 0\n\t"
      "v_mfma_f32_16x16x32_bf16 v[132:135], v[80:83], a[112:115], v[132:135]\n\t"
      "v_mfma_f32_16x16x32_bf16 v[132:135], v[84:87], a[116:119], v[132:135]\n\t"
      "v_mfma_f32_16x16x32_bf16 v[136:139], v[76:79], a[120:123], 0\n\t"
      "s_nop 3\n\t"
      "v_max_f32 v128, v128, v129\n\t"
      "v_max3_f32 v128, v128, v130, v131\n\t"
      "v_mfma_f32_16x16x32_bf16 v[136:139], v[80:83], a[124:127], v[136:139]\n\t"
      "v_sub_f32 v184, 0, v128\n\t"
      "v_sub_f32 v185, 0, v128\n\t"
      "v_mfma_f32_16x16x32_bf16 v[136:139], v[84:87], a[128:131], v[136:139]\n\t"
      "v_sub_f32 v186, 0, v128\n\t"
      "v_sub_f32 v187, 0, v128\n\t"
      "v_mfma_f32_16x16x32_bf16 v[140:143], v[76:79], a[132:135], 0\n\t"
      "v_max_f32 v132, v132, v133\n\t"
      "v_max3_f32 v132, v132, v134, v135\n\t"
      "v_mfma_f32_16x16x32_bf16 v[140:143], v[80:83], a[136:139], v[140:143]\n\t"
      "v_sub_f32 v188, 0, v132\n\t"
      "v_sub_f32 v189, 0, v132\n\t"
      "v_mfma_f32_16x16x32_bf16 v[140:143], v[84:87], a[140:143], v[140:143]\n\t"
      "v_sub_f32 v190, 0, v132\n\t"
      "v_sub_f32 v191, 0, v132\n\t"
      "v_mfma_f32_16x16x32_bf16 v[144:147], v[76:79], a[144:147], 0\n\t"
      "v_max_f32 v136, v136, v137\n\t"
      "v_max3_f32 v136, v136, v138, v139\n\t"
      "v_mfma_f32_16x16x32_bf16 v[144:147], v[80:83], a[148:151], v[144:147]\n\t"
      "v_sub_f32 v192, 0, v136\n\t"
      "v_sub_f32 v193, 0, v136\n\t"
      "v_mfma_f32_16x16x32_bf16 v[144:147], v[84:87], a[152:155], v[144:147]\n\t"
      "v_sub_f32 v194, 0, v136\n\t"
      "v_sub_f32 v195, 0, v136\n\t"
      "v_mfma_f32_16x16x32_bf16 v[148:151], v[76:79], a[156:159], 0\n\t"
      "v_max_f32 v140, v140, v141\n\t"
      "v_max3_f32 v140, v140, v142, v143\n\t"
      "v_mfma_f32_16x16x32_bf16 v[148:151], v[80:83], a[160:163], v[148:151]\n\t"
      "v_sub_f32 v196, 0, v140\n\t"
      "v_sub_f32 v197, 0, v140\n\t"
      "v_mfma_f32_16x16x32_bf16 v[148:151], v[84:87], a[164:167], v[148:151]\n\t"
      "v_sub_f32 v198, 0, v140\n\t"
      "v_sub_f32 v199, 0, v140\n\t"
      "v_mfma_f32_16x16x32_bf16 v[152:155], v[76:79], a[168:171], 0\n\t"
      "v_max_f32 v144, v144, v145\n\t"
      "v_max3_f32 v144, v144, v146, v147\n\t"
      "v_mfma_f32_16x16x32_bf16 v[152:155], v[80:83], a[172:175], v[152:155]\n\t"
      "v_sub_f32 v200, 0, v144\n\t"
      "v_sub_f32 v201, 0, v144\n\t"
      "v_mfma_f32_16x16x32_bf16 v[152:155], v[84:87], a[176:179], v[152:155]\n\t"
      "v_sub_f32 v202, 0, v144\n\t"
      "v_sub_f32 v203, 0, v144\n\t"
      "v_max_f32 v148, v148, v149\n\t"
      "v_max3_f32 v148, v148, v150, v151\n\t"
      "v_sub_f32 v204, 0, v148\n\t"
      "v_sub_f32 v205, 0, v148\n\t"
      "v_sub_f32 v206, 0, v148\n\t"
      "v_sub_f32 v207, 0, v148\n\t"
      "v_max_f32 v152, v152, v153\n\t"
      "v_max3_f32 v152, v152, v154, v155\n\t"
      "v_sub_f32 v208, 0, v152\n\t"
      "v_sub_f32 v209, 0, v152\n\t"
      "v_sub_f32 v210, 0, v152\n\t"
      "v_sub_f32 v211, 0, v152\n\t"
      "s_sleep 12\n\t"
      "global_load_dword v222, v235, s[40:41] sc1\n\t"
      "s_sleep 4\n\t"
      "Lp0a%=:\n\t"
      "global_load_dword v233, v235, s[40:41] sc1\n\t"
      "s_waitcnt vmcnt(1)\n\t"
      "v_cmp_ne_u32 vcc, s54, v222\n\t"
      "s_add_u32 s57, s57, 1\n\t"
      "s_cbranch_vccz Lp0b%=\n\t"
      "global_load_dword v222, v235, s[40:41] sc1\n\t"
      "s_waitcnt vmcnt(1)\n\t"
      "v_cmp_ne_u32 vcc, s54, v233\n\t"
      "s_cmp_lt_u32 s57, 0x100000\n\t"
      "s_cbranch_vccz Lp0b%=\n\t"
      "s_cbranch_scc1 Lp0a%=\n\t"
      "Lp0b%=:\n\t"
      "global_load_lds_dwordx4 v219, s[58:59] offset:-3072 sc1\n\t"
      "global_load_lds_dwordx4 v219, s[58:59] offset:-2048 sc1\n\t"
      "global_load_lds_dwordx4 v219, s[58:59] offset:-1024 sc1\n\t"
      "global_load_lds_dwordx4 v219, s[58:59] offset:0 sc1\n\t"
      "global_load_lds_dwordx4 v219, s[58:59] offset:1024 sc1\n\t"
      "global_load_lds_dwordx4 v219, s[58:59] offset:2048 sc1\n\t"
      "s_mov_b32 m0, s43\n\t"
      "s_nop 0\n\t"
      "global_load_lds_dwordx4 v220, s[58:59] offset:-3072 sc1\n\t"
      "global_load_lds_dwordx4 v220, s[58:59] offset:-2048 sc1\n\t"
      "global_load_lds_dwordx4 v220, s[58:59] offset:-1024 sc1\n\t"
      "global_load_lds_dwordx4 v220, s[58:59] offset:0 sc1\n\t"
      "global_load_lds_dwordx4 v220, s[58:59] offset:1024 sc1\n\t"
      "global_load_lds_dwordx4 v220, s[58:59] offset:2048 sc1\n\t"
      "Lhave0%=:\n\t"
      "global_load_dword v222, v224, s[40:41] sc1\n\t"
      "s_waitcnt vmcnt(7)\n\t"
      "s_barrier\n\t"
      "s_cmp_eq_u32 s35, 0\n\t"
      "s_cbranch_scc1 Lheavy%=\n\t"
      "ds_read_b128 a[180:183], v217 offset:0\n\t"
      "ds_read_b128 a[184:187], v217 offset:1024\n\t"
      "ds_read_b128 a[188:191], v217 offset:2048\n\t"
      "ds_read_b128 a[200:203], v217 offset:5120\n\t"
      "ds_read_b128 a[196:199], v217 offset:4096\n\t"
      "ds_read_b128 a[192:195], v217 offset:3072\n\t"
      "ds_read_b128 a[204:207], v218 offset:0\n\t"
      "ds_read_b128 a[208:211], v218 offset:1024\n\t"
      "ds_read_b128 a[212:215], v218 offset:2048\n\t"
      "ds_read_b128 a[224:227], v218 offset:5120\n\t"
      "ds_read_b128 a[220:223], v218 offset:4096\n\t"
      "ds_read_b128 a[216:219], v218 offset:3072\n\t"
      "s_xor_b32 s42, s42, 0x10000\n\t"
      "s_add_u32 s58, s58, 57344\n\t"
      "s_addc_u32 s59, s59, 0\n\t"
      "s_mov_b32 m0, s42\n\t"
      "s_xor_b32 s42, s42, 0x10000\n\t"
      "s_waitcnt lgkmcnt(9)\n\t"
      "v_mfma_f32_16x16x32_bf16 v[124:127], a[180:183], a[96:99], v[184:187]\n\t"
      "v_mfma_f32_16x16x32_bf16 v[124:127], a[184:187], a[100:103], v[124:127]\n\t"
      "v_mfma_f32_16x16x32_bf16 v[124:127], a[188:191], a[104:107], v[124:127]\n\t"
      "s_waitcnt lgkmcnt(6)\n\t"
      "v_mfma_f32_16x16x32_bf16 v[128:131], a[200:203], a[104:107], v[184:187]\n\t"
      "v_mfma_f32_16x16x32_bf16 v[128:131], a[196:199], a[100:103], v[128:131]\n\t"
      "v_mfma_f32_16x16x32_bf16 v[128:131], a[192:195], a[96:99], v[128:131]\n\t"
      "s_waitcnt lgkmcnt(3)\n\t"
      "v_mfma_f32_16x16x32_bf16 v[132:135], a[204:207], a[96:99], v[184:187]\n\t"
      "v_mfma_f32_16x16x32_bf16 v[132:135], a[208:211], a[100:103], v[132:135]\n\t"
      "v_mfma_f32_16x16x32_bf16 v[132:135], a[212:215], a[104:107], v[132:135]\n\t"
      "s_waitcnt lgkmcnt(0)\n\t"
      "v_exp_f32 v124, v124\n\t"
      "v_mfma_f32_16x16x32_bf16 v[136:139], a[224:227], a[104:107], v[184:187]\n\t"
      "v_exp_f32 v125, v125\n\t"
      "v_mfma_f32_16x16x32_bf16 v[136:139], a[220:223], a[100:103], v[136:139]\n\t"
      "v_mfma_f32_16x16x32_bf16 v[136:139], a[216:219], a[96:99], v[136:139]\n\t"
      "Lp1La%=:\n\t"
      "s_waitcnt vmcnt(0)\n\t"
      "v_cmp_ne_u32 vcc, s54, v222\n\t"
      "s_add_u32 s57, s57, 1\n\t"
      "s_and_b32 s48, vcc_lo, 0xff00\n\t"
      "s_cbranch_scc0 Lp1Lb%=\n\t"
      "global_load_dword v222, v224, s[40:41] sc1\n\t"
      "s_cmp_lt_u32 s57, 0x100000\n\t"
      "s_cbranch_scc1 Lp1La%=\n\t"
      "Lp1Lb%=:\n\t"
      "s_barrier\n\t"
      "ds_read_b128 v[100:103], v218 offset:6144\n\t"
      "ds_read_b128 v[76:79], v217 offset:6144\n\t"
      "ds_read_b128 v[80:83], v217 offset:7168\n\t"
      "ds_read_b128 v[104:107], v218 offset:7168\n\t"
      "ds_read_b128 v[108:111], v218 offset:8192\n\t"
      "ds_read_b128 v[84:87], v217 offset:8192\n\t"
      "ds_read_b128 v[88:91], v217 offset:9216\n\t"
      "ds_read_b128 v[112:115], v218 offset:9216\n\t"
      "v_exp_f32 v126, v126\n\t"
      "v_exp_f32 v127, v127\n\t"
      "v_exp_f32 v128, v128\n\t"
      "v_exp_f32 v129, v129\n\t"
      "v_exp_f32 v130, v130\n\t"
      "v_exp_f32 v131, v131\n\t"
      "v_exp_f32 v132, v132\n\t"
      "v_exp_f32 v133, v133\n\t"
      "v_exp_f32 v134, v134\n\t"
      "v_exp_f32 v135, v135\n\t"
      "v_cvt_pk_bf16_f32 v164, v124, v125\n\t"
      "v_cvt_pk_bf16_f32 v165, v126, v127\n\t"
      "v_mfma_f32_16x16x32_bf16 v[140:143], a[180:183], a[132:135], v[196:199]\n\t"
      "global_load_lds_dwordx4 v219, s[58:59] offset:-3072 sc1\n\t"
      "v_cvt_pk_bf16_f32 v166, v128, v129\n\t"
      "v_cvt_pk_bf16_f32 v167, v130, v131\n\t"
      "v_mfma_f32_16x16x32_bf16 v[140:143], a[184:187], a[136:139], v[140:143]\n\t"
      "v_exp_f32 v136, v136\n\t"
      "v_mfma_f32_16x16x32_bf16 v[140:143], a[188:191], a[140:143], v[140:143]\n\t"
      "global_load_lds_dwordx4 v219, s[58:59] offset:-2048 sc1\n\t"
      "v_exp_f32 v137, v137\n\t"
      "v_mfma_f32_16x16x32_bf16 v[144:147], a[200:203], a[140:143], v[196:199]\n\t"
      "v_exp_f32 v138, v138\n\t"
      "v_mfma_f32_16x16x32_bf16 v[144:147], a[196:199], a[136:139], v[144:147]\n\t"
      "global_load_lds_dwordx4 v219, s[58:59] offset:-1024 sc1\n\t"
      "v_exp_f32 v139, v139\n\t"
      "v_mfma_f32_16x16x32_bf16 v[144:147], a[192:195], a[132:135], v[144:147]\n\t"
      "v_cvt_pk_bf16_f32 v168, v132, v133\n\t"
      "v_cvt_pk_bf16_f32 v169, v134, v135\n\t"
      "v_mfma_f32_16x16x32_bf16 v[148:151], a[204:207], a[132:135], v[196:199]\n\t"
      "global_load_lds_dwordx4 v219, s[58:59] offset:0 sc1\n\t"
      "v_exp_f32 v140, v140\n\t"
      "v_mfma_f32_16x16x32_bf16 v[148:151], a[208:211], a[136:139], v[148:151]\n\t"
      "v_exp_f32 v141, v141\n\t"
      "v_mfma_f32_16x16x32_bf16 v[148:151], a[212:215], a[140:143], v[148:151]\n\t"
      "global_load_lds_dwordx4 v219, s[58:59] offset:1024 sc1\n\t"
      "v_exp_f32 v142, v142\n\t"
      "v_mfma_f32_16x16x32_bf16 v[152:155], a[224:227], a[140:143], v[196:199]\n\t"
      "v_exp_f32 v143, v143\n\t"
      "v_mfma_f32_16x16x32_bf16 v[152:155], a[220:223], a[136:139], v[152:155]\n\t"
      "global_load_lds_dwordx4 v219, s[58:59] offset:2048 sc1\n\t"
      "v_exp_f32 v144, v144\n\t"
      "v_mfma_f32_16x16x32_bf16 v[152:155], a[216:219], a[132:135], v[152:155]\n\t"
      "v_exp_f32 v145, v145\n\t"
      "v_exp_f32 v146, v146\n\t"
      "v_exp_f32 v147, v147\n\t"
      "v_exp_f32 v148, v148\n\t"
      "v_exp_f32 v149, v149\n\t"
      "v_exp_f32 v150, v150\n\t"
      "v_exp_f32 v151, v151\n\t"
      "s_nop 0\n\t"
      "v_exp_f32 v152, v152\n\t"
      "v_cvt_pk_bf16_f32 v170, v136, v137\n\t"
      "v_cvt_pk_bf16_f32 v171, v138, v139\n\t"
      "s_mov_b64 exec, 1\n\t"
      "global_load_dword v239, %1, s[22:23]\n\t"
      "s_mov_b64 exec, -1\n\t"
      ".p2align 6\n\t"
      "Lmain_loopL%=:\n\t"
      "v_mfma_f32_16x16x32_bf16 v[124:127], a[180:183], a[108:111], v[188:191]\n\t"
      "ds_read_b128 v[116:119], v218 offset:10240\n\t"
      "ds_read_b128 v[92:95], v217 offset:10240\n\t"
      "v_mfma_f32_16x16x32_bf16 v[124:127], a[184:187], a[112:115], v[124:127]\n\t"
      "v_cvt_pk_bf16_f32 v172, v140, v141\n\t"
      "v_cvt_pk_bf16_f32 v173, v142, v143\n\t"
      "v_mfma_f32_16x16x32_bf16 v[124:127], a[188:191], a[116:119], v[124:127]\n\t"
      "ds_read_b128 v[96:99], v217 offset:11264\n\t"
      "ds_read_b128 v[120:123], v218 offset:11264\n\t"
      "v_mfma_f32_16x16x32_bf16 v[128:131], a[200:203], a[116:119], v[188:191]\n\t"
      "v_cvt_pk_bf16_f32 v174, v144, v145\n\t"
      "v_cvt_pk_bf16_f32 v175, v146, v147\n\t"
      "v_mfma_f32_16x16x32_bf16 v[128:131], a[196:199], a[112:115], v[128:131]\n\t"
      "s_add_u32 m0, m0, 6144\n\t"
      "v_cvt_pk_bf16_f32 v176, v148, v149\n\t"
      "v_mfma_f32_16x16x32_bf16 v[128:131], a[192:195], a[108:111], v[128:131]\n\t"
      "global_load_lds_dwordx4 v220, s[58:59] offset:-3072 sc1\n\t"
      "v_cvt_pk_bf16_f32 v177, v150, v151\n\t"
      "v_mfma_f32_16x16x32_bf16 v[132:135], a[204:207], a[108:111], v[188:191]\n\t"
      "v_exp_f32 v153, v153\n\t"
      "v_mfma_f32_16x16x32_bf16 v[132:135], a[208:211], a[112:115], v[132:135]\n\t"
      "v_exp_f32 v154, v154\n\t"
      "v_mfma_f32_16x16x32_bf16 v[132:135], a[212:215], a[116:119], v[132:135]\n\t"
      "v_exp_f32 v155, v155\n\t"
      "v_mfma_f32_16x16x32_bf16 v[136:139], a[224:227], a[116:119], v[188:191]\n\t"
      "global_load_lds_dwordx4 v220, s[58:59] offset:-2048 sc1\n\t"
      "v_cvt_pk_bf16_f32 v178, v152, v153\n\t"
      "v_mfma_f32_16x16x32_bf16 v[136:139], a[220:223], a[112:115], v[136:139]\n\t"
      "v_exp_f32 v124, v124\n\t"
      "v_mfma_f32_16x16x32_bf16 v[136:139], a[216:219], a[108:111], v[136:139]\n\t"
      "v_exp_f32 v125, v125\n\t"
      "v_smfmac_f32_16x16x64_bf16 a[228:231], v[212:215], v[164:171], v216\n\t"
      "v_xor_b32 v217, 0x10000, v217\n\t"
      "s_waitcnt lgkmcnt(0)\n\t"
      "v_mfma_f32_16x16x32_bf16 a[0:3], v[100:103], v[168:171], a[0:3]\n\t"
      "global_load_lds_dwordx4 v220, s[58:59] offset:-1024 sc1\n\t"
      "v_cvt_pk_bf16_f32 v179, v154, v155\n\t"
      "v_mfma_f32_16x16x32_bf16 a[0:3], v[76:79], v[164:167], a[0:3]\n\t"
      "v_exp_f32 v126, v126\n\t"
      "v_mfma_f32_16x16x32_bf16 a[4:7], v[80:83], v[164:167], a[4:7]\n\t"
      "v_exp_f32 v127, v127\n\t"
      "v_mfma_f32_16x16x32_bf16 a[4:7], v[104:107], v[168:171], a[4:7]\n\t"
      "v_exp_f32 v128, v128\n\t"
      "v_mfma_f32_16x16x32_bf16 a[8:11], v[108:111], v[168:171], a[8:11]\n\t"
      "global_load_lds_dwordx4 v220, s[58:59] offset:0 sc1\n\t"
      "v_mfma_f32_16x16x32_bf16 a[8:11], v[84:87], v[164:167], a[8:11]\n\t"
      "v_exp_f32 v129, v129\n\t"
      "v_mfma_f32_16x16x32_bf16 a[12:15], v[88:91], v[164:167], a[12:15]\n\t"
      "v_exp_f32 v130, v130\n\t"
      "v_mfma_f32_16x16x32_bf16 a[12:15], v[112:115], v[168:171], a[12:15]\n\t"
      "v_exp_f32 v131, v131\n\t"
      "v_mfma_f32_16x16x32_bf16 a[16:19], v[116:119], v[168:171], a[16:19]\n\t"
      "global_load_lds_dwordx4 v220, s[58:59] offset:1024 sc1\n\t"
      "v_mfma_f32_16x16x32_bf16 a[16:19], v[92:95], v[164:167], a[16:19]\n\t"
      "v_exp_f32 v132, v132\n\t"
      "v_mfma_f32_16x16x32_bf16 a[20:23], v[96:99], v[164:167], a[20:23]\n\t"
      "v_exp_f32 v133, v133\n\t"
      "v_mfma_f32_16x16x32_bf16 a[20:23], v[120:123], v[168:171], a[20:23]\n\t"
      "v_cvt_pk_bf16_f32 v164, v124, v125\n\t"
      "v_cvt_pk_bf16_f32 v165, v126, v127\n\t"
      "v_mfma_f32_16x16x32_bf16 v[140:143], a[180:183], a[144:147], v[200:203]\n\t"
      "global_load_lds_dwordx4 v220, s[58:59] offset:2048 sc1\n\t"
      "v_cvt_pk_bf16_f32 v166, v128, v129\n\t"
      "v_mfma_f32_16x16x32_bf16 v[140:143], a[184:187], a[148:151], v[140:143]\n\t"
      "s_add_u32 s58, s58, 57344\n\t"
      "s_addc_u32 s59, s59, 0\n\t"
      "v_mfma_f32_16x16x32_bf16 v[140:143], a[188:191], a[152:155], v[140:143]\n\t"
      "v_exp_f32 v134, v134\n\t"
      "v_mfma_f32_16x16x32_bf16 v[144:147], a[200:203], a[152:155], v[200:203]\n\t"
      "v_exp_f32 v135, v135\n\t"
      "v_mfma_f32_16x16x32_bf16 v[144:147], a[196:199], a[148:151], v[144:147]\n\t"
      "global_load_dword v222, v221, s[58:59] sc1\n\t"
      "v_mfma_f32_16x16x32_bf16 v[144:147], a[192:195], a[144:147], v[144:147]\n\t"
      "v_exp_f32 v136, v136\n\t"
      "v_mfma_f32_16x16x32_bf16 v[148:151], a[204:207], a[144:147], v[200:203]\n\t"
      "v_exp_f32 v137, v137\n\t"
      "v_mfma_f32_16x16x32_bf16 v[148:151], a[208:211], a[148:151], v[148:151]\n\t"
      "v_xor_b32 v218, 0x10000, v218\n\t"
      "v_cvt_pk_bf16_f32 v167, v130, v131\n\t"
      "v_mfma_f32_16x16x32_bf16 v[148:151], a[212:215], a[152:155], v[148:151]\n\t"
      "v_exp_f32 v138, v138\n\t"
      "v_mfma_f32_16x16x32_bf16 v[152:155], a[224:227], a[152:155], v[200:203]\n\t"
      "v_exp_f32 v139, v139\n\t"
      "v_mfma_f32_16x16x32_bf16 v[152:155], a[220:223], a[148:151], v[152:155]\n\t"
      "v_cvt_pk_bf16_f32 v168, v132, v133\n\t"
      "v_cvt_pk_bf16_f32 v169, v134, v135\n\t"
      "v_mfma_f32_16x16x32_bf16 v[152:155], a[216:219], a[144:147], v[152:155]\n\t"
      "v_exp_f32 v140, v140\n\t"
      "v_smfmac_f32_16x16x64_bf16 a[240:243], v[212:215], v[172:179], v216\n\t"
      "v_exp_f32 v141, v141\n\t"
      "v_mfma_f32_16x16x32_bf16 v[4:7], v[100:103], v[176:179], v[4:7]\n\t"
      "v_exp_f32 v142, v142\n\t"
      "v_mfma_f32_16x16x32_bf16 v[4:7], v[76:79], v[172:175], v[4:7]\n\t"
      "v_exp_f32 v143, v143\n\t"
      "v_mfma_f32_16x16x32_bf16 v[8:11], v[80:83], v[172:175], v[8:11]\n\t"
      "v_exp_f32 v144, v144\n\t"
      "v_mfma_f32_16x16x32_bf16 v[8:11], v[104:107], v[176:179], v[8:11]\n\t"
      "v_exp_f32 v145, v145\n\t"
      "v_mfma_f32_16x16x32_bf16 v[12:15], v[108:111], v[176:179], v[12:15]\n\t"
      "v_exp_f32 v146, v146\n\t"
      "v_mfma_f32_16x16x32_bf16 v[12:15], v[84:87], v[172:175], v[12:15]\n\t"
      "v_exp_f32 v147, v147\n\t"
      "v_mfma_f32_16x16x32_bf16 v[16:19], v[88:91], v[172:175], v[16:19]\n\t"
      "v_exp_f32 v148, v148\n\t"
      "v_mfma_f32_16x16x32_bf16 v[16:19], v[112:115], v[176:179], v[16:19]\n\t"
      "v_exp_f32 v149, v149\n\t"
      "v_mfma_f32_16x16x32_bf16 v[20:23], v[116:119], v[176:179], v[20:23]\n\t"
      "v_exp_f32 v150, v150\n\t"
      "v_mfma_f32_16x16x32_bf16 v[20:23], v[92:95], v[172:175], v[20:23]\n\t"
      "v_exp_f32 v151, v151\n\t"
      "v_mfma_f32_16x16x32_bf16 v[24:27], v[96:99], v[172:175], v[24:27]\n\t"
      "v_exp_f32 v152, v152\n\t"
      "v_mfma_f32_16x16x32_bf16 v[24:27], v[120:123], v[176:179], v[24:27]\n\t"
      "v_cvt_pk_bf16_f32 v170, v136, v137\n\t"
      "v_cvt_pk_bf16_f32 v171, v138, v139\n\t"
      "v_mfma_f32_16x16x32_bf16 v[124:127], a[180:183], a[120:123], v[192:195]\n\t"
      "v_cvt_pk_bf16_f32 v172, v140, v141\n\t"
      "v_cvt_pk_bf16_f32 v173, v142, v143\n\t"
      "v_mfma_f32_16x16x32_bf16 v[124:127], a[184:187], a[124:127], v[124:127]\n\t"
      "v_cvt_pk_bf16_f32 v174, v144, v145\n\t"
      "v_cvt_pk_bf16_f32 v175, v146, v147\n\t"
      "v_mfma_f32_16x16x32_bf16 v[124:127], a[188:191], a[128:131], v[124:127]\n\t"
      "v_exp_f32 v153, v153\n\t"
      "v_mfma_f32_16x16x32_bf16 v[128:131], a[200:203], a[128:131], v[192:195]\n\t"
      "v_exp_f32 v154, v154\n\t"
      "v_mfma_f32_16x16x32_bf16 v[128:131], a[196:199], a[124:127], v[128:131]\n\t"
      "v_exp_f32 v155, v155\n\t"
      "v_mfma_f32_16x16x32_bf16 v[128:131], a[192:195], a[120:123], v[128:131]\n\t"
      "v_cvt_pk_bf16_f32 v176, v148, v149\n\t"
      "v_cvt_pk_bf16_f32 v177, v150, v151\n\t"
      "v_mfma_f32_16x16x32_bf16 v[132:135], a[204:207], a[120:123], v[192:195]\n\t"
      "v_exp_f32 v124, v124\n\t"
      "v_mfma_f32_16x16x32_bf16 v[132:135], a[208:211], a[124:127], v[132:135]\n\t"
      "v_exp_f32 v125, v125\n\t"
      "v_mfma_f32_16x16x32_bf16 v[132:135], a[212:215], a[128:131], v[132:135]\n\t"
      "v_exp_f32 v126, v126\n\t"
      "v_mfma_f32_16x16x32_bf16 v[136:139], a[224:227], a[128:131], v[192:195]\n\t"
      "v_exp_f32 v127, v127\n\t"
      "v_mfma_f32_16x16x32_bf16 v[136:139], a[220:223], a[124:127], v[136:139]\n\t"
      "v_exp_f32 v128, v128\n\t"
      "v_mfma_f32_16x16x32_bf16 v[136:139], a[216:219], a[120:123], v[136:139]\n\t"
      "v_exp_f32 v129, v129\n\t"
      "v_smfmac_f32_16x16x64_bf16 a[232:235], v[212:215], v[164:171], v216\n\t"
      "v_exp_f32 v130, v130\n\t"
      "v_mfma_f32_16x16x32_bf16 a[24:27], v[100:103], v[168:171], a[24:27]\n\t"
      "v_exp_f32 v131, v131\n\t"
      "v_mfma_f32_16x16x32_bf16 a[24:27], v[76:79], v[164:167], a[24:27]\n\t"
      "v_exp_f32 v132, v132\n\t"
      "v_mfma_f32_16x16x32_bf16 a[28:31], v[80:83], v[164:167], a[28:31]\n\t"
      "v_exp_f32 v133, v133\n\t"
      "v_mfma_f32_16x16x32_bf16 a[28:31], v[104:107], v[168:171], a[28:31]\n\t"
      "v_exp_f32 v134, v134\n\t"
      "v_mfma_f32_16x16x32_bf16 a[32:35], v[108:111], v[168:171], a[32:35]\n\t"
      "v_exp_f32 v135, v135\n\t"
      "v_mfma_f32_16x16x32_bf16 a[32:35], v[84:87], v[164:167], a[32:35]\n\t"
      "v_exp_f32 v136, v136\n\t"
      "v_mfma_f32_16x16x32_bf16 a[36:39], v[88:91], v[164:167], a[36:39]\n\t"
      "v_exp_f32 v137, v137\n\t"
      "v_mfma_f32_16x16x32_bf16 a[36:39], v[112:115], v[168:171], a[36:39]\n\t"
      "v_exp_f32 v138, v138\n\t"
      "v_mfma_f32_16x16x32_bf16 a[40:43], v[116:119], v[168:171], a[40:43]\n\t"
      "v_exp_f32 v139, v139\n\t"
      "v_mfma_f32_16x16x32_bf16 a[40:43], v[92:95], v[164:167], a[40:43]\n\t"
      "v_cvt_pk_bf16_f32 v178, v152, v153\n\t"
      "v_cvt_pk_bf16_f32 v179, v154, v155\n\t"
      "v_mfma_f32_16x16x32_bf16 a[44:47], v[96:99], v[164:167], a[44:47]\n\t"
      "v_mfma_f32_16x16x32_bf16 a[44:47], v[120:123], v[168:171], a[44:47]\n\t"
      "v_cvt_pk_bf16_f32 v164, v124, v125\n\t"
      "v_cvt_pk_bf16_f32 v165, v126, v127\n\t"
      "v_mfma_f32_16x16x32_bf16 v[140:143], a[180:183], a[156:159], v[204:207]\n\t"
      "v_cvt_pk_bf16_f32 v166, v128, v129\n\t"
      "v_cvt_pk_bf16_f32 v167, v130, v131\n\t"
      "v_mfma_f32_16x16x32_bf16 v[140:143], a[184:187], a[160:163], v[140:143]\n\t"
      "v_cvt_pk_bf16_f32 v168, v132, v133\n\t"
      "v_cvt_pk_bf16_f32 v169, v134, v135\n\t"
      "v_mfma_f32_16x16x32_bf16 v[140:143], a[188:191], a[164:167], v[140:143]\n\t"
      "s_waitcnt vmcnt(0)\n\t"
      "s_barrier\n\t"
      "v_mfma_f32_16x16x32_bf16 v[144:147], a[200:203], a[164:167], v[204:207]\n\t"
      "ds_read_b128 a[180:183], v217 offset:0\n\t"
      "ds_read_b128 a[184:187], v217 offset:1024\n\t"
      "v_mfma_f32_16x16x32_bf16 v[144:147], a[196:199], a[160:163], v[144:147]\n\t"
      "v_cvt_pk_bf16_f32 v170, v136, v137\n\t"
      "v_cvt_pk_bf16_f32 v171, v138, v139\n\t"
      "v_mfma_f32_16x16x32_bf16 v[144:147], a[192:195], a[156:159], v[144:147]\n\t"
      "ds_read_b128 a[188:191], v217 offset:2048\n\t"
      "ds_read_b128 a[200:203], v217 offset:5120\n\t"
      "v_mfma_f32_16x16x32_bf16 v[148:151], a[204:207], a[156:159], v[204:207]\n\t"
      "v_exp_f32 v140, v140\n\t"
      "v_mfma_f32_16x16x32_bf16 v[148:151], a[208:211], a[160:163], v[148:151]\n\t"
      "v_exp_f32 v141, v141\n\t"
      "v_mfma_f32_16x16x32_bf16 v[148:151], a[212:215], a[164:167], v[148:151]\n\t"
      "ds_read_b128 a[196:199], v217 offset:4096\n\t"
      "ds_read_b128 a[192:195], v217 offset:3072\n\t"
      "v_mfma_f32_16x16x32_bf16 v[152:155], a[224:227], a[164:167], v[204:207]\n\t"
      "v_exp_f32 v142, v142\n\t"
      "v_mfma_f32_16x16x32_bf16 v[152:155], a[220:223], a[160:163], v[152:155]\n\t"
      "v_exp_f32 v143, v143\n\t"
      "v_mfma_f32_16x16x32_bf16 v[152:155], a[216:219], a[156:159], v[152:155]\n\t"
      "ds_read_b128 a[204:207], v218 offset:0\n\t"
      "ds_read_b128 a[208:211], v218 offset:1024\n\t"
      "v_smfmac_f32_16x16x64_bf16 a[244:247], v[212:215], v[172:179], v216\n\t"
      "v_exp_f32 v144, v144\n\t"
      "v_mfma_f32_16x16x32_bf16 v[28:31], v[100:103], v[176:179], v[28:31]\n\t"
      "ds_read_b128 a[212:215], v218 offset:2048\n\t"
      "ds_read_b128 a[224:227], v218 offset:5120\n\t"
      "v_mfma_f32_16x16x32_bf16 v[28:31], v[76:79], v[172:175], v[28:31]\n\t"
      "v_exp_f32 v145, v145\n\t"
      "v_mfma_f32_16x16x32_bf16 v[32:35], v[80:83], v[172:175], v[32:35]\n\t"
      "ds_read_b128 a[220:223], v218 offset:4096\n\t"
      "ds_read_b128 a[216:219], v218 offset:3072\n\t"
      "v_mfma_f32_16x16x32_bf16 v[32:35], v[104:107], v[176:179], v[32:35]\n\t"
      "v_exp_f32 v146, v146\n\t"
      "v_mfma_f32_16x16x32_bf16 v[36:39], v[108:111], v[176:179], v[36:39]\n\t"
      "v_exp_f32 v147, v147\n\t"
      "v_mfma_f32_16x16x32_bf16 v[36:39], v[84:87], v[172:175], v[36:39]\n\t"
      "v_exp_f32 v148, v148\n\t"
      "v_mfma_f32_16x16x32_bf16 v[40:43], v[88:91], v[172:175], v[40:43]\n\t"
      "v_exp_f32 v149, v149\n\t"
      "v_mfma_f32_16x16x32_bf16 v[40:43], v[112:115], v[176:179], v[40:43]\n\t"
      "v_exp_f32 v150, v150\n\t"
      "v_mfma_f32_16x16x32_bf16 v[44:47], v[116:119], v[176:179], v[44:47]\n\t"
      "v_cmp_ne_u32 vcc, s54, v222\n\t"
      "s_mov_b32 m0, s42\n\t"
      "v_mfma_f32_16x16x32_bf16 v[44:47], v[92:95], v[172:175], v[44:47]\n\t"
      "s_xor_b32 s42, s42, 0x10000\n\t"
      "s_cbranch_vccnz LslowL%=\n\t"
      "LbackL%=:\n\t"
      "v_mfma_f32_16x16x32_bf16 v[48:51], v[96:99], v[172:175], v[48:51]\n\t"
      "v_exp_f32 v151, v151\n\t"
      "v_mfma_f32_16x16x32_bf16 v[48:51], v[120:123], v[176:179], v[48:51]\n\t"
      "v_cvt_pk_bf16_f32 v172, v140, v141\n\t"
      "s_waitcnt lgkmcnt(6)\n\t"
      "v_mfma_f32_16x16x32_bf16 v[124:127], a[180:183], a[96:99], v[184:187]\n\t"
      "v_cvt_pk_bf16_f32 v173, v142, v143\n\t"
      "v_cvt_pk_bf16_f32 v174, v144, v145\n\t"
      "v_mfma_f32_16x16x32_bf16 v[124:127], a[184:187], a[100:103], v[124:127]\n\t"
      "global_load_lds_dwordx4 v219, s[58:59] offset:-3072 sc1\n\t"
      "v_cvt_pk_bf16_f32 v175, v146, v147\n\t"
      "v_mfma_f32_16x16x32_bf16 v[124:127], a[188:191], a[104:107], v[124:127]\n\t"
      "v_exp_f32 v152, v152\n\t"
      "v_mfma_f32_16x16x32_bf16 v[128:131], a[200:203], a[104:107], v[184:187]\n\t"
      "v_exp_f32 v153, v153\n\t"
      "v_mfma_f32_16x16x32_bf16 v[128:131], a[196:199], a[100:103], v[128:131]\n\t"
      "v_exp_f32 v154, v154\n\t"
      "v_mfma_f32_16x16x32_bf16 v[128:131], a[192:195], a[96:99], v[128:131]\n\t"
      "v_cvt_pk_bf16_f32 v176, v148, v149\n\t"
      "s_waitcnt lgkmcnt(0)\n\t"
      "v_mfma_f32_16x16x32_bf16 v[132:135], a[204:207], a[96:99], v[184:187]\n\t"
      "global_load_lds_dwordx4 v219, s[58:59] offset:-2048 sc1\n\t"
      "v_cvt_pk_bf16_f32 v177, v150, v151\n\t"
      "v_mfma_f32_16x16x32_bf16 v[132:135], a[208:211], a[100:103], v[132:135]\n\t"
      "v_exp_f32 v155, v155\n\t"
      "v_mfma_f32_16x16x32_bf16 v[132:135], a[212:215], a[104:107], v[132:135]\n\t"
      "v_exp_f32 v124, v124\n\t"
      "v_mfma_f32_16x16x32_bf16 v[136:139], a[224:227], a[104:107], v[184:187]\n\t"
      "v_exp_f32 v125, v125\n\t"
      "v_mfma_f32_16x16x32_bf16 v[136:139], a[220:223], a[100:103], v[136:139]\n\t"
      "global_load_lds_dwordx4 v219, s[58:59] offset:-1024 sc1\n\t"
      "v_cvt_pk_bf16_f32 v178, v152, v153\n\t"
      "v_mfma_f32_16x16x32_bf16 v[136:139], a[216:219], a[96:99], v[136:139]\n\t"
      "v_exp_f32 v126, v126\n\t"
      "v_smfmac_f32_16x16x64_bf16 a[236:239], v[212:215], v[164:171], v216\n\t"
      "v_exp_f32 v127, v127\n\t"
      "v_mfma_f32_16x16x32_bf16 a[48:51], v[100:103], v[168:171], a[48:51]\n\t"
      "v_exp_f32 v128, v128\n\t"
      "v_mfma_f32_16x16x32_bf16 a[48:51], v[76:79], v[164:167], a[48:51]\n\t"
      "global_load_lds_dwordx4 v219, s[58:59] offset:0 sc1\n\t"
      "v_cvt_pk_bf16_f32 v179, v154, v155\n\t"
      "v_mfma_f32_16x16x32_bf16 a[52:55], v[80:83], v[164:167], a[52:55]\n\t"
      "v_exp_f32 v129, v129\n\t"
      "v_mfma_f32_16x16x32_bf16 a[52:55], v[104:107], v[168:171], a[52:55]\n\t"
      "v_exp_f32 v130, v130\n\t"
      "v_mfma_f32_16x16x32_bf16 a[56:59], v[108:111], v[168:171], a[56:59]\n\t"
      "v_exp_f32 v131, v131\n\t"
      "v_mfma_f32_16x16x32_bf16 a[56:59], v[84:87], v[164:167], a[56:59]\n\t"
      "global_load_lds_dwordx4 v219, s[58:59] offset:1024 sc1\n\t"
      "v_mfma_f32_16x16x32_bf16 a[60:63], v[88:91], v[164:167], a[60:63]\n\t"
      "v_exp_f32 v132, v132\n\t"
      "v_mfma_f32_16x16x32_bf16 a[60:63], v[112:115], v[168:171], a[60:63]\n\t"
      "v_exp_f32 v133, v133\n\t"
      "v_mfma_f32_16x16x32_bf16 a[64:67], v[116:119], v[168:171], a[64:67]\n\t"
      "v_exp_f32 v134, v134\n\t"
      "v_mfma_f32_16x16x32_bf16 a[64:67], v[92:95], v[164:167], a[64:67]\n\t"
      "global_load_lds_dwordx4 v219, s[58:59] offset:2048 sc1\n\t"
      "v_mfma_f32_16x16x32_bf16 a[68:71], v[96:99], v[164:167], a[68:71]\n\t"
      "v_exp_f32 v135, v135\n\t"
      "v_mfma_f32_16x16x32_bf16 a[68:71], v[120:123], v[168:171], a[68:71]\n\t"
      "v_cvt_pk_bf16_f32 v164, v124, v125\n\t"
      "v_cvt_pk_bf16_f32 v165, v126, v127\n\t"
      "v_mfma_f32_16x16x32_bf16 v[140:143], a[180:183], a[132:135], v[196:199]\n\t"
      "v_cvt_pk_bf16_f32 v166, v128, v129\n\t"
      "v_cvt_pk_bf16_f32 v167, v130, v131\n\t"
      "v_mfma_f32_16x16x32_bf16 v[140:143], a[184:187], a[136:139], v[140:143]\n\t"
      "v_exp_f32 v136, v136\n\t"
      "v_mfma_f32_16x16x32_bf16 v[140:143], a[188:191], a[140:143], v[140:143]\n\t"
      "v_exp_f32 v137, v137\n\t"
      "v_mfma_f32_16x16x32_bf16 v[144:147], a[200:203], a[140:143], v[196:199]\n\t"
      "v_exp_f32 v138, v138\n\t"
      "v_mfma_f32_16x16x32_bf16 v[144:147], a[196:199], a[136:139], v[144:147]\n\t"
      "v_exp_f32 v139, v139\n\t"
      "v_mfma_f32_16x16x32_bf16 v[144:147], a[192:195], a[132:135], v[144:147]\n\t"
      "v_cvt_pk_bf16_f32 v168, v132, v133\n\t"
      "v_cvt_pk_bf16_f32 v169, v134, v135\n\t"
      "v_mfma_f32_16x16x32_bf16 v[148:151], a[204:207], a[132:135], v[196:199]\n\t"
      "v_exp_f32 v140, v140\n\t"
      "v_mfma_f32_16x16x32_bf16 v[148:151], a[208:211], a[136:139], v[148:151]\n\t"
      "v_exp_f32 v141, v141\n\t"
      "v_mfma_f32_16x16x32_bf16 v[148:151], a[212:215], a[140:143], v[148:151]\n\t"
      "v_exp_f32 v142, v142\n\t"
      "v_mfma_f32_16x16x32_bf16 v[152:155], a[224:227], a[140:143], v[196:199]\n\t"
      "v_exp_f32 v143, v143\n\t"
      "v_mfma_f32_16x16x32_bf16 v[152:155], a[220:223], a[136:139], v[152:155]\n\t"
      "v_exp_f32 v144, v144\n\t"
      "v_mfma_f32_16x16x32_bf16 v[152:155], a[216:219], a[132:135], v[152:155]\n\t"
      "v_exp_f32 v145, v145\n\t"
      "v_smfmac_f32_16x16x64_bf16 a[248:251], v[212:215], v[172:179], v216\n\t"
      "v_exp_f32 v146, v146\n\t"
      "v_mfma_f32_16x16x32_bf16 v[52:55], v[100:103], v[176:179], v[52:55]\n\t"
      "v_exp_f32 v147, v147\n\t"
      "v_mfma_f32_16x16x32_bf16 v[52:55], v[76:79], v[172:175], v[52:55]\n\t"
      "v_exp_f32 v148, v148\n\t"
      "v_mfma_f32_16x16x32_bf16 v[56:59], v[80:83], v[172:175], v[56:59]\n\t"
      "v_exp_f32 v149, v149\n\t"
      "v_mfma_f32_16x16x32_bf16 v[56:59], v[104:107], v[176:179], v[56:59]\n\t"
      "ds_read_b128 v[100:103], v218 offset:6144\n\t"
      "ds_read_b128 v[76:79], v217 offset:6144\n\t"
      "v_mfma_f32_16x16x32_bf16 v[60:63], v[108:111], v[176:179], v[60:63]\n\t"
      "v_exp_f32 v150, v150\n\t"
      "v_mfma_f32_16x16x32_bf16 v[60:63], v[84:87], v[172:175], v[60:63]\n\t"
      "ds_read_b128 v[80:83], v217 offset:7168\n\t"
      "ds_read_b128 v[104:107], v218 offset:7168\n\t"
      "v_mfma_f32_16x16x32_bf16 v[64:67], v[88:91], v[172:175], v[64:67]\n\t"
      "v_exp_f32 v151, v151\n\t"
      "v_mfma_f32_16x16x32_bf16 v[64:67], v[112:115], v[176:179], v[64:67]\n\t"
      "ds_read_b128 v[108:111], v218 offset:8192\n\t"
      "ds_read_b128 v[84:87], v217 offset:8192\n\t"
      "v_mfma_f32_16x16x32_bf16 v[68:71], v[116:119], v[176:179], v[68:71]\n\t"
      "v_exp_f32 v152, v152\n\t"
      "v_mfma_f32_16x16x32_bf16 v[68:71], v[92:95], v[172:175], v[68:71]\n\t"
      "ds_read_b128 v[88:91], v217 offset:9216\n\t"
      "ds_read_b128 v[112:115], v218 offset:9216\n\t"
      "v_mfma_f32_16x16x32_bf16 v[72:75], v[96:99], v[172:175], v[72:75]\n\t"
      "s_sub_u32 s44, s44, 1\n\t"
      "v_cvt_pk_bf16_f32 v170, v136, v137\n\t"
      "v_mfma_f32_16x16x32_bf16 v[72:75], v[120:123], v[176:179], v[72:75]\n\t"
      "v_cvt_pk_bf16_f32 v171, v138, v139\n\t"
      "s_cbranch_scc0 Lmain_loopL%=\n\t"
      "v_mfma_f32_16x16x32_bf16 v[124:127], a[180:183], a[108:111], v[188:191]\n\t"
      "ds_read_b128 v[116:119], v218 offset:10240\n\t"
      "ds_read_b128 v[92:95], v217 offset:10240\n\t"
      "v_mfma_f32_16x16x32_bf16 v[124:127], a[184:187], a[112:115], v[124:127]\n\t"
      "v_cvt_pk_bf16_f32 v172, v140, v141\n\t"
      "v_cvt_pk_bf16_f32 v173, v142, v143\n\t"
      "v_mfma_f32_16x16x32_bf16 v[124:127], a[188:191], a[116:119], v[124:127]\n\t"
      "ds_read_b128 v[96:99], v217 offset:11264\n\t"
      "ds_read_b128 v[120:123], v218 offset:11264\n\t"
      "v_mfma_f32_16x16x32_bf16 v[128:131], a[200:203], a[116:119], v[188:191]\n\t"
      "v_cvt_pk_bf16_f32 v174, v144, v145\n\t"
      "v_cvt_pk_bf16_f32 v175, v146, v147\n\t"
      "v_mfma_f32_16x16x32_bf16 v[128:131], a[196:199], a[112:115], v[128:131]\n\t"
      "s_add_u32 m0, m0, 6144\n\t"
      "v_cvt_pk_bf16_f32 v176, v148, v149\n\t"
      "v_mfma_f32_16x16x32_bf16 v[128:131], a[192:195], a[108:111], v[128:131]\n\t"
      "global_load_lds_dwordx4 v220, s[58:59] offset:-3072 sc1\n\t"
      "v_cvt_pk_bf16_f32 v177, v150, v151\n\t"
      "v_mfma_f32_16x16x32_bf16 v[132:135], a[204:207], a[108:111], v[188:191]\n\t"
      "v_exp_f32 v153, v153\n\t"
      "v_mfma_f32_16x16x32_bf16 v[132:135], a[208:211], a[112:115], v[132:135]\n\t"
      "v_exp_f32 v154, v154\n\t"
      "v_mfma_f32_16x16x32_bf16 v[132:135], a[212:215], a[116:119], v[132:135]\n\t"
      "v_exp_f32 v155, v155\n\t"
      "v_mfma_f32_16x16x32_bf16 v[136:139], a[224:227], a[116:119], v[188:191]\n\t"
      "global_load_lds_dwordx4 v220, s[58:59] offset:-2048 sc1\n\t"
      "v_cvt_pk_bf16_f32 v178, v152, v153\n\t"
      "v_mfma_f32_16x16x32_bf16 v[136:139], a[220:223], a[112:115], v[136:139]\n\t"
      "v_exp_f32 v124, v124\n\t"
      "v_mfma_f32_16x16x32_bf16 v[136:139], a[216:219], a[108:111], v[136:139]\n\t"
      "v_exp_f32 v125, v125\n\t"
      "v_smfmac_f32_16x16x64_bf16 a[228:231], v[212:215], v[164:171], v216\n\t"
      "v_xor_b32 v217, 0x10000, v217\n\t"
      "s_waitcnt lgkmcnt(0)\n\t"
      "v_mfma_f32_16x16x32_bf16 a[0:3], v[100:103], v[168:171], a[0:3]\n\t"
      "global_load_lds_dwordx4 v220, s[58:59] offset:-1024 sc1\n\t"
      "v_cvt_pk_bf16_f32 v179, v154, v155\n\t"
      "v_mfma_f32_16x16x32_bf16 a[0:3], v[76:79], v[164:167], a[0:3]\n\t"
      "v_exp_f32 v126, v126\n\t"
      "v_mfma_f32_16x16x32_bf16 a[4:7], v[80:83], v[164:167], a[4:7]\n\t"
      "v_exp_f32 v127, v127\n\t"
      "v_mfma_f32_16x16x32_bf16 a[4:7], v[104:107], v[168:171], a[4:7]\n\t"
      "v_exp_f32 v128, v128\n\t"
      "v_mfma_f32_16x16x32_bf16 a[8:11], v[108:111], v[168:171], a[8:11]\n\t"
      "global_load_lds_dwordx4 v220, s[58:59] offset:0 sc1\n\t"
      "v_mfma_f32_16x16x32_bf16 a[8:11], v[84:87], v[164:167], a[8:11]\n\t"
      "v_exp_f32 v129, v129\n\t"
      "v_mfma_f32_16x16x32_bf16 a[12:15], v[88:91], v[164:167], a[12:15]\n\t"
      "v_exp_f32 v130, v130\n\t"
      "v_mfma_f32_16x16x32_bf16 a[12:15], v[112:115], v[168:171], a[12:15]\n\t"
      "v_exp_f32 v131, v131\n\t"
      "v_mfma_f32_16x16x32_bf16 a[16:19], v[116:119], v[168:171], a[16:19]\n\t"
      "global_load_lds_dwordx4 v220, s[58:59] offset:1024 sc1\n\t"
      "v_mfma_f32_16x16x32_bf16 a[16:19], v[92:95], v[164:167], a[16:19]\n\t"
      "v_exp_f32 v132, v132\n\t"
      "v_mfma_f32_16x16x32_bf16 a[20:23], v[96:99], v[164:167], a[20:23]\n\t"
      "v_exp_f32 v133, v133\n\t"
      "v_mfma_f32_16x16x32_bf16 a[20:23], v[120:123], v[168:171], a[20:23]\n\t"
      "v_cvt_pk_bf16_f32 v164, v124, v125\n\t"
      "v_cvt_pk_bf16_f32 v165, v126, v127\n\t"
      "v_mfma_f32_16x16x32_bf16 v[140:143], a[180:183], a[144:147], v[200:203]\n\t"
      "global_load_lds_dwordx4 v220, s[58:59] offset:2048 sc1\n\t"
      "v_cvt_pk_bf16_f32 v166, v128, v129\n\t"
      "v_mfma_f32_16x16x32_bf16 v[140:143], a[184:187], a[148:151], v[140:143]\n\t"
      "v_mfma_f32_16x16x32_bf16 v[140:143], a[188:191], a[152:155], v[140:143]\n\t"
      "v_exp_f32 v134, v134\n\t"
      "v_mfma_f32_16x16x32_bf16 v[144:147], a[200:203], a[152:155], v[200:203]\n\t"
      "v_exp_f32 v135, v135\n\t"
      "v_mfma_f32_16x16x32_bf16 v[144:147], a[196:199], a[148:151], v[144:147]\n\t"
      "v_mfma_f32_16x16x32_bf16 v[144:147], a[192:195], a[144:147], v[144:147]\n\t"
      "v_exp_f32 v136, v136\n\t"
      "v_mfma_f32_16x16x32_bf16 v[148:151], a[204:207], a[144:147], v[200:203]\n\t"
      "v_exp_f32 v137, v137\n\t"
      "v_mfma_f32_16x16x32_bf16 v[148:151], a[208:211], a[148:151], v[148:151]\n\t"
      "v_xor_b32 v218, 0x10000, v218\n\t"
      "v_cvt_pk_bf16_f32 v167, v130, v131\n\t"
      "v_mfma_f32_16x16x32_bf16 v[148:151], a[212:215], a[152:155], v[148:151]\n\t"
      "v_exp_f32 v138, v138\n\t"
      "v_mfma_f32_16x16x32_bf16 v[152:155], a[224:227], a[152:155], v[200:203]\n\t"
      "v_exp_f32 v139, v139\n\t"
      "v_mfma_f32_16x16x32_bf16 v[152:155], a[220:223], a[148:151], v[152:155]\n\t"
      "v_cvt_pk_bf16_f32 v168, v132, v133\n\t"
      "v_cvt_pk_bf16_f32 v169, v134, v135\n\t"
      "v_mfma_f32_16x16x32_bf16 v[152:155], a[216:219], a[144:147], v[152:155]\n\t"
      "v_exp_f32 v140, v140\n\t"
      "v_smfmac_f32_16x16x64_bf16 a[240:243], v[212:215], v[172:179], v216\n\t"
      "v_exp_f32 v141, v141\n\t"
      "v_mfma_f32_16x16x32_bf16 v[4:7], v[100:103], v[176:179], v[4:7]\n\t"
      "v_exp_f32 v142, v142\n\t"
      "v_mfma_f32_16x16x32_bf16 v[4:7], v[76:79], v[172:175], v[4:7]\n\t"
      "v_exp_f32 v143, v143\n\t"
      "v_mfma_f32_16x16x32_bf16 v[8:11], v[80:83], v[172:175], v[8:11]\n\t"
      "v_exp_f32 v144, v144\n\t"
      "v_mfma_f32_16x16x32_bf16 v[8:11], v[104:107], v[176:179], v[8:11]\n\t"
      "v_exp_f32 v145, v145\n\t"
      "v_mfma_f32_16x16x32_bf16 v[12:15], v[108:111], v[176:179], v[12:15]\n\t"
      "v_exp_f32 v146, v146\n\t"
      "v_mfma_f32_16x16x32_bf16 v[12:15], v[84:87], v[172:175], v[12:15]\n\t"
      "v_exp_f32 v147, v147\n\t"
      "v_mfma_f32_16x16x32_bf16 v[16:19], v[88:91], v[172:175], v[16:19]\n\t"
      "v_exp_f32 v148, v148\n\t"
      "v_mfma_f32_16x16x32_bf16 v[16:19], v[112:115], v[176:179], v[16:19]\n\t"
      "v_exp_f32 v149, v149\n\t"
      "v_mfma_f32_16x16x32_bf16 v[20:23], v[116:119], v[176:179], v[20:23]\n\t"
      "v_exp_f32 v150, v150\n\t"
      "v_mfma_f32_16x16x32_bf16 v[20:23], v[92:95], v[172:175], v[20:23]\n\t"
      "v_exp_f32 v151, v151\n\t"
      "v_mfma_f32_16x16x32_bf16 v[24:27], v[96:99], v[172:175], v[24:27]\n\t"
      "v_exp_f32 v152, v152\n\t"
      "v_mfma_f32_16x16x32_bf16 v[24:27], v[120:123], v[176:179], v[24:27]\n\t"
      "v_cvt_pk_bf16_f32 v170, v136, v137\n\t"
      "v_cvt_pk_bf16_f32 v171, v138, v139\n\t"
      "v_mfma_f32_16x16x32_bf16 v[124:127], a[180:183], a[120:123], v[192:195]\n\t"
      "v_cvt_pk_bf16_f32 v172, v140, v141\n\t"
      "v_cvt_pk_bf16_f32 v173, v142, v143\n\t"
      "v_mfma_f32_16x16x32_bf16 v[124:127], a[184:187], a[124:127], v[124:127]\n\t"
      "v_cvt_pk_bf16_f32 v174, v144, v145\n\t"
      "v_cvt_pk_bf16_f32 v175, v146, v147\n\t"
      "v_mfma_f32_16x16x32_bf16 v[124:127], a[188:191], a[128:131], v[124:127]\n\t"
      "v_exp_f32 v153, v153\n\t"
      "v_mfma_f32_16x16x32_bf16 v[128:131], a[200:203], a[128:131], v[192:195]\n\t"
      "v_exp_f32 v154, v154\n\t"
      "v_mfma_f32_16x16x32_bf16 v[128:131], a[196:199], a[124:127], v[128:131]\n\t"
      "v_exp_f32 v155, v155\n\t"
      "v_mfma_f32_16x16x32_bf16 v[128:131], a[192:195], a[120:123], v[128:131]\n\t"
      "v_cvt_pk_bf16_f32 v176, v148, v149\n\t"
      "v_cvt_pk_bf16_f32 v177, v150, v151\n\t"
      "v_mfma_f32_16x16x32_bf16 v[132:135], a[204:207], a[120:123], v[192:195]\n\t"
      "v_exp_f32 v124, v124\n\t"
      "v_mfma_f32_16x16x32_bf16 v[132:135], a[208:211], a[124:127], v[132:135]\n\t"
      "v_exp_f32 v125, v125\n\t"
      "v_mfma_f32_16x16x32_bf16 v[132:135], a[212:215], a[128:131], v[132:135]\n\t"
      "v_exp_f32 v126, v126\n\t"
      "v_mfma_f32_16x16x32_bf16 v[136:139], a[224:227], a[128:131], v[192:195]\n\t"
      "v_exp_f32 v127, v127\n\t"
      "v_mfma_f32_16x16x32_bf16 v[136:139], a[220:223], a[124:127], v[136:139]\n\t"
      "v_exp_f32 v128, v128\n\t"
      "v_mfma_f32_16x16x32_bf16 v[136:139], a[216:219], a[120:123], v[136:139]\n\t"
      "v_exp_f32 v129, v129\n\t"
      "v_smfmac_f32_16x16x64_bf16 a[232:235], v[212:215], v[164:171], v216\n\t"
      "v_exp_f32 v130, v130\n\t"
      "v_mfma_f32_16x16x32_bf16 a[24:27], v[100:103], v[168:171], a[24:27]\n\t"
      "v_exp_f32 v131, v131\n\t"
      "v_mfma_f32_16x16x32_bf16 a[24:27], v[76:79], v[164:167], a[24:27]\n\t"
      "v_exp_f32 v132, v132\n\t"
      "v_mfma_f32_16x16x32_bf16 a[28:31], v[80:83], v[164:167], a[28:31]\n\t"
      "v_exp_f32 v133, v133\n\t"
      "v_mfma_f32_16x16x32_bf16 a[28:31], v[104:107], v[168:171], a[28:31]\n\t"
      "v_exp_f32 v134, v134\n\t"
      "v_mfma_f32_16x16x32_bf16 a[32:35], v[108:111], v[168:171], a[32:35]\n\t"
      "v_exp_f32 v135, v135\n\t"
      "v_mfma_f32_16x16x32_bf16 a[32:35], v[84:87], v[164:167], a[32:35]\n\t"
      "v_exp_f32 v136, v136\n\t"
      "v_mfma_f32_16x16x32_bf16 a[36:39], v[88:91], v[164:167], a[36:39]\n\t"
      "v_exp_f32 v137, v137\n\t"
      "v_mfma_f32_16x16x32_bf16 a[36:39], v[112:115], v[168:171], a[36:39]\n\t"
      "v_exp_f32 v138, v138\n\t"
      "v_mfma_f32_16x16x32_bf16 a[40:43], v[116:119], v[168:171], a[40:43]\n\t"
      "v_exp_f32 v139, v139\n\t"
      "v_mfma_f32_16x16x32_bf16 a[40:43], v[92:95], v[164:167], a[40:43]\n\t"
      "v_cvt_pk_bf16_f32 v178, v152, v153\n\t"
      "v_cvt_pk_bf16_f32 v179, v154, v155\n\t"
      "v_mfma_f32_16x16x32_bf16 a[44:47], v[96:99], v[164:167], a[44:47]\n\t"
      "v_mfma_f32_16x16x32_bf16 a[44:47], v[120:123], v[168:171], a[44:47]\n\t"
      "v_cvt_pk_bf16_f32 v164, v124, v125\n\t"
      "v_cvt_pk_bf16_f32 v165, v126, v127\n\t"
      "v_mfma_f32_16x16x32_bf16 v[140:143], a[180:183], a[156:159], v[204:207]\n\t"
      "v_cvt_pk_bf16_f32 v166, v128, v129\n\t"
      "v_cvt_pk_bf16_f32 v167, v130, v131\n\t"
      "v_mfma_f32_16x16x32_bf16 v[140:143], a[184:187], a[160:163], v[140:143]\n\t"
      "v_cvt_pk_bf16_f32 v168, v132, v133\n\t"
      "v_cvt_pk_bf16_f32 v169, v134, v135\n\t"
      "v_mfma_f32_16x16x32_bf16 v[140:143], a[188:191], a[164:167], v[140:143]\n\t"
      "s_waitcnt vmcnt(0)\n\t"
      "s_barrier\n\t"
      "v_mfma_f32_16x16x32_bf16 v[144:147], a[200:203], a[164:167], v[204:207]\n\t"
      "ds_read_b128 a[180:183], v217 offset:0\n\t"
      "ds_read_b128 a[184:187], v217 offset:1024\n\t"
      "v_mfma_f32_16x16x32_bf16 v[144:147], a[196:199], a[160:163], v[144:147]\n\t"
      "v_cvt_pk_bf16_f32 v170, v136, v137\n\t"
      "v_cvt_pk_bf16_f32 v171, v138, v139\n\t"
      "v_mfma_f32_16x16x32_bf16 v[144:147], a[192:195], a[156:159], v[144:147]\n\t"
      "ds_read_b128 a[188:191], v217 offset:2048\n\t"
      "ds_read_b128 a[200:203], v217 offset:5120\n\t"
      "v_mfma_f32_16x16x32_bf16 v[148:151], a[204:207], a[156:159], v[204:207]\n\t"
      "v_exp_f32 v140, v140\n\t"
      "v_mfma_f32_16x16x32_bf16 v[148:151], a[208:211], a[160:163], v[148:151]\n\t"
      "v_exp_f32 v141, v141\n\t"
      "v_mfma_f32_16x16x32_bf16 v[148:151], a[212:215], a[164:167], v[148:151]\n\t"
      "ds_read_b128 a[196:199], v217 offset:4096\n\t"
      "ds_read_b128 a[192:195], v217 offset:3072\n\t"
      "v_mfma_f32_16x16x32_bf16 v[152:155], a[224:227], a[164:167], v[204:207]\n\t"
      "v_exp_f32 v142, v142\n\t"
      "v_mfma_f32_16x16x32_bf16 v[152:155], a[220:223], a[160:163], v[152:155]\n\t"
      "v_exp_f32 v143, v143\n\t"
      "v_mfma_f32_16x16x32_bf16 v[152:155], a[216:219], a[156:159], v[152:155]\n\t"
      "ds_read_b128 a[204:207], v218 offset:0\n\t"
      "ds_read_b128 a[208:211], v218 offset:1024\n\t"
      "v_smfmac_f32_16x16x64_bf16 a[244:247], v[212:215], v[172:179], v216\n\t"
      "v_exp_f32 v144, v144\n\t"
      "v_mfma_f32_16x16x32_bf16 v[28:31], v[100:103], v[176:179], v[28:31]\n\t"
      "ds_read_b128 a[212:215], v218 offset:2048\n\t"
      "ds_read_b128 a[224:227], v218 offset:5120\n\t"
      "v_mfma_f32_16x16x32_bf16 v[28:31], v[76:79], v[172:175], v[28:31]\n\t"
      "v_exp_f32 v145, v145\n\t"
      "v_mfma_f32_16x16x32_bf16 v[32:35], v[80:83], v[172:175], v[32:35]\n\t"
      "ds_read_b128 a[220:223], v218 offset:4096\n\t"
      "ds_read_b128 a[216:219], v218 offset:3072\n\t"
      "v_mfma_f32_16x16x32_bf16 v[32:35], v[104:107], v[176:179], v[32:35]\n\t"
      "v_exp_f32 v146, v146\n\t"
      "v_mfma_f32_16x16x32_bf16 v[36:39], v[108:111], v[176:179], v[36:39]\n\t"
      "v_exp_f32 v147, v147\n\t"
      "v_mfma_f32_16x16x32_bf16 v[36:39], v[84:87], v[172:175], v[36:39]\n\t"
      "v_exp_f32 v148, v148\n\t"
      "v_mfma_f32_16x16x32_bf16 v[40:43], v[88:91], v[172:175], v[40:43]\n\t"
      "v_exp_f32 v149, v149\n\t"
      "v_mfma_f32_16x16x32_bf16 v[40:43], v[112:115], v[176:179], v[40:43]\n\t"
      "v_exp_f32 v150, v150\n\t"
      "v_mfma_f32_16x16x32_bf16 v[44:47], v[116:119], v[176:179], v[44:47]\n\t"
      "v_mfma_f32_16x16x32_bf16 v[44:47], v[92:95], v[172:175], v[44:47]\n\t"
      "v_mfma_f32_16x16x32_bf16 v[48:51], v[96:99], v[172:175], v[48:51]\n\t"
      "v_exp_f32 v151, v151\n\t"
      "v_mfma_f32_16x16x32_bf16 v[48:51], v[120:123], v[176:179], v[48:51]\n\t"
      "v_cvt_pk_bf16_f32 v172, v140, v141\n\t"
      "s_waitcnt lgkmcnt(6)\n\t"
      "v_mfma_f32_16x16x32_bf16 v[124:127], a[180:183], a[96:99], v[184:187]\n\t"
      "v_cvt_pk_bf16_f32 v173, v142, v143\n\t"
      "v_cvt_pk_bf16_f32 v174, v144, v145\n\t"
      "v_mfma_f32_16x16x32_bf16 v[124:127], a[184:187], a[100:103], v[124:127]\n\t"
      "v_cvt_pk_bf16_f32 v175, v146, v147\n\t"
      "v_mfma_f32_16x16x32_bf16 v[124:127], a[188:191], a[104:107], v[124:127]\n\t"
      "v_exp_f32 v152, v152\n\t"
      "v_mfma_f32_16x16x32_bf16 v[128:131], a[200:203], a[104:107], v[184:187]\n\t"
      "v_exp_f32 v153, v153\n\t"
      "v_mfma_f32_16x16x32_bf16 v[128:131], a[196:199], a[100:103], v[128:131]\n\t"
      "v_exp_f32 v154, v154\n\t"
      "v_mfma_f32_16x16x32_bf16 v[128:131], a[192:195], a[96:99], v[128:131]\n\t"
      "v_cvt_pk_bf16_f32 v176, v148, v149\n\t"
      "s_waitcnt lgkmcnt(0)\n\t"
      "v_mfma_f32_16x16x32_bf16 v[132:135], a[204:207], a[96:99], v[184:187]\n\t"
      "v_cvt_pk_bf16_f32 v177, v150, v151\n\t"
      "v_mfma_f32_16x16x32_bf16 v[132:135], a[208:211], a[100:103], v[132:135]\n\t"
      "v_exp_f32 v155, v155\n\t"
      "v_mfma_f32_16x16x32_bf16 v[132:135], a[212:215], a[104:107], v[132:135]\n\t"
      "v_exp_f32 v124, v124\n\t"
      "v_mfma_f32_16x16x32_bf16 v[136:139], a[224:227], a[104:107], v[184:187]\n\t"
      "v_exp_f32 v125, v125\n\t"
      "v_mfma_f32_16x16x32_bf16 v[136:139], a[220:223], a[100:103], v[136:139]\n\t"
      "v_cvt_pk_bf16_f32 v178, v152, v153\n\t"
      "v_mfma_f32_16x16x32_bf16 v[136:139], a[216:219], a[96:99], v[136:139]\n\t"
      "v_exp_f32 v126, v126\n\t"
      "v_smfmac_f32_16x16x64_bf16 a[236:239], v[212:215], v[164:171], v216\n\t"
      "v_exp_f32 v127, v127\n\t"
      "v_mfma_f32_16x16x32_bf16 a[48:51], v[100:103], v[168:171], a[48:51]\n\t"
      "v_exp_f32 v128, v128\n\t"
      "v_mfma_f32_16x16x32_bf16 a[48:51], v[76:79], v[164:167], a[48:51]\n\t"
      "v_cvt_pk_bf16_f32 v179, v154, v155\n\t"
      "v_mfma_f32_16x16x32_bf16 a[52:55], v[80:83], v[164:167], a[52:55]\n\t"
      "v_exp_f32 v129, v129\n\t"
      "v_mfma_f32_16x16x32_bf16 a[52:55], v[104:107], v[168:171], a[52:55]\n\t"
      "v_exp_f32 v130, v130\n\t"
      "v_mfma_f32_16x16x32_bf16 a[56:59], v[108:111], v[168:171], a[56:59]\n\t"
      "v_exp_f32 v131, v131\n\t"
      "v_mfma_f32_16x16x32_bf16 a[56:59], v[84:87], v[164:167], a[56:59]\n\t"
      "v_mfma_f32_16x16x32_bf16 a[60:63], v[88:91], v[164:167], a[60:63]\n\t"
      "v_exp_f32 v132, v132\n\t"
      "v_mfma_f32_16x16x32_bf16 a[60:63], v[112:115], v[168:171], a[60:63]\n\t"
      "v_exp_f32 v133, v133\n\t"
      "v_mfma_f32_16x16x32_bf16 a[64:67], v[116:119], v[168:171], a[64:67]\n\t"
      "v_exp_f32 v134, v134\n\t"
      "v_mfma_f32_16x16x32_bf16 a[64:67], v[92:95], v[164:167], a[64:67]\n\t"
      "v_mfma_f32_16x16x32_bf16 a[68:71], v[96:99], v[164:167], a[68:71]\n\t"
      "v_exp_f32 v135, v135\n\t"
      "v_mfma_f32_16x16x32_bf16 a[68:71], v[120:123], v[168:171], a[68:71]\n\t"
      "v_cvt_pk_bf16_f32 v164, v124, v125\n\t"
      "v_cvt_pk_bf16_f32 v165, v126, v127\n\t"
      "v_mfma_f32_16x16x32_bf16 v[140:143], a[180:183], a[132:135], v[196:199]\n\t"
      "v_cvt_pk_bf16_f32 v166, v128, v129\n\t"
      "v_cvt_pk_bf16_f32 v167, v130, v131\n\t"
      "v_mfma_f32_16x16x32_bf16 v[140:143], a[184:187], a[136:139], v[140:143]\n\t"
      "v_exp_f32 v136, v136\n\t"
      "v_mfma_f32_16x16x32_bf16 v[140:143], a[188:191], a[140:143], v[140:143]\n\t"
      "v_exp_f32 v137, v137\n\t"
      "v_mfma_f32_16x16x32_bf16 v[144:147], a[200:203], a[140:143], v[196:199]\n\t"
      "v_exp_f32 v138, v138\n\t"
      "v_mfma_f32_16x16x32_bf16 v[144:147], a[196:199], a[136:139], v[144:147]\n\t"
      "v_exp_f32 v139, v139\n\t"
      "v_mfma_f32_16x16x32_bf16 v[144:147], a[192:195], a[132:135], v[144:147]\n\t"
      "v_cvt_pk_bf16_f32 v168, v132, v133\n\t"
      "v_cvt_pk_bf16_f32 v169, v134, v135\n\t"
      "v_mfma_f32_16x16x32_bf16 v[148:151], a[204:207], a[132:135], v[196:199]\n\t"
      "v_exp_f32 v140, v140\n\t"
      "v_mfma_f32_16x16x32_bf16 v[148:151], a[208:211], a[136:139], v[148:151]\n\t"
      "v_exp_f32 v141, v141\n\t"
      "v_mfma_f32_16x16x32_bf16 v[148:151], a[212:215], a[140:143], v[148:151]\n\t"
      "v_exp_f32 v142, v142\n\t"
      "v_mfma_f32_16x16x32_bf16 v[152:155], a[224:227], a[140:143], v[196:199]\n\t"
      "v_exp_f32 v143, v143\n\t"
      "v_mfma_f32_16x16x32_bf16 v[152:155], a[220:223], a[136:139], v[152:155]\n\t"
      "v_exp_f32 v144, v144\n\t"
      "v_mfma_f32_16x16x32_bf16 v[152:155], a[216:219], a[132:135], v[152:155]\n\t"
      "v_exp_f32 v145, v145\n\t"
      "v_smfmac_f32_16x16x64_bf16 a[248:251], v[212:215], v[172:179], v216\n\t"
      "v_exp_f32 v146, v146\n\t"
      "v_mfma_f32_16x16x32_bf16 v[52:55], v[100:103], v[176:179], v[52:55]\n\t"
      "v_exp_f32 v147, v147\n\t"
      "v_mfma_f32_16x16x32_bf16 v[52:55], v[76:79], v[172:175], v[52:55]\n\t"
      "v_exp_f32 v148, v148\n\t"
      "v_mfma_f32_16x16x32_bf16 v[56:59], v[80:83], v[172:175], v[56:59]\n\t"
      "v_exp_f32 v149, v149\n\t"
      "v_mfma_f32_16x16x32_bf16 v[56:59], v[104:107], v[176:179], v[56:59]\n\t"
      "ds_read_b128 v[100:103], v218 offset:6144\n\t"
      "ds_read_b128 v[76:79], v217 offset:6144\n\t"
      "v_mfma_f32_16x16x32_bf16 v[60:63], v[108:111], v[176:179], v[60:63]\n\t"
      "v_exp_f32 v150, v150\n\t"
      "v_mfma_f32_16x16x32_bf16 v[60:63], v[84:87], v[172:175], v[60:63]\n\t"
      "ds_read_b128 v[80:83], v217 offset:7168\n\t"
      "ds_read_b128 v[104:107], v218 offset:7168\n\t"
      "v_mfma_f32_16x16x32_bf16 v[64:67], v[88:91], v[172:175], v[64:67]\n\t"
      "v_exp_f32 v151, v151\n\t"
      "v_mfma_f32_16x16x32_bf16 v[64:67], v[112:115], v[176:179], v[64:67]\n\t"
      "ds_read_b128 v[108:111], v218 offset:8192\n\t"
      "ds_read_b128 v[84:87], v217 offset:8192\n\t"
      "v_mfma_f32_16x16x32_bf16 v[68:71], v[116:119], v[176:179], v[68:71]\n\t"
      "v_exp_f32 v152, v152\n\t"
      "v_mfma_f32_16x16x32_bf16 v[68:71], v[92:95], v[172:175], v[68:71]\n\t"
      "ds_read_b128 v[88:91], v217 offset:9216\n\t"
      "ds_read_b128 v[112:115], v218 offset:9216\n\t"
      "v_mfma_f32_16x16x32_bf16 v[72:75], v[96:99], v[172:175], v[72:75]\n\t"
      "v_cvt_pk_bf16_f32 v170, v136, v137\n\t"
      "v_mfma_f32_16x16x32_bf16 v[72:75], v[120:123], v[176:179], v[72:75]\n\t"
      "v_cvt_pk_bf16_f32 v171, v138, v139\n\t"
      "v_mfma_f32_16x16x32_bf16 v[124:127], a[180:183], a[108:111], v[188:191]\n\t"
      "ds_read_b128 v[116:119], v218 offset:10240\n\t"
      "ds_read_b128 v[92:95], v217 offset:10240\n\t"
      "v_mov_b32 v223, 524544\n\t"
      "global_load_dword v223, v223, s[40:41] sc1\n\t"
      "s_mov_b32 s36, 0x55555555\n\t"
      "s_mov_b32 s37, 0x55555555\n\t"
      "v_mfma_f32_16x16x32_bf16 v[124:127], a[184:187], a[112:115], v[124:127]\n\t"
      "v_cvt_pk_bf16_f32 v172, v140, v141\n\t"
      "v_cvt_pk_bf16_f32 v173, v142, v143\n\t"
      "v_mfma_f32_16x16x32_bf16 v[124:127], a[188:191], a[116:119], v[124:127]\n\t"
      "ds_read_b128 v[96:99], v217 offset:11264\n\t"
      "ds_read_b128 v[120:123], v218 offset:11264\n\t"
      "v_mfma_f32_16x16x32_bf16 v[128:131], a[200:203], a[116:119], v[188:191]\n\t"
      "v_cvt_pk_bf16_f32 v174, v144, v145\n\t"
      "v_cvt_pk_bf16_f32 v175, v146, v147\n\t"
      "v_mfma_f32_16x16x32_bf16 v[128:131], a[196:199], a[112:115], v[128:131]\n\t"
      "v_cvt_pk_bf16_f32 v176, v148, v149\n\t"
      "v_mfma_f32_16x16x32_bf16 v[128:131], a[192:195], a[108:111], v[128:131]\n\t"
      "v_cvt_pk_bf16_f32 v177, v150, v151\n\t"
      "v_mfma_f32_16x16x32_bf16 v[132:135], a[204:207], a[108:111], v[188:191]\n\t"
      "v_exp_f32 v153, v153\n\t"
      "v_mfma_f32_16x16x32_bf16 v[132:135], a[208:211], a[112:115], v[132:135]\n\t"
      "v_exp_f32 v154, v154\n\t"
      "v_mfma_f32_16x16x32_bf16 v[132:135], a[212:215], a[116:119], v[132:135]\n\t"
      "v_exp_f32 v155, v155\n\t"
      "v_mfma_f32_16x16x32_bf16 v[136:139], a[224:227], a[116:119], v[188:191]\n\t"
      "v_cvt_pk_bf16_f32 v178, v152, v153\n\t"
      "v_mfma_f32_16x16x32_bf16 v[136:139], a[220:223], a[112:115], v[136:139]\n\t"
      "v_exp_f32 v124, v124\n\t"
      "v_mfma_f32_16x16x32_bf16 v[136:139], a[216:219], a[108:111], v[136:139]\n\t"
      "v_exp_f32 v125, v125\n\t"
      "v_smfmac_f32_16x16x64_bf16 a[228:231], v[212:215], v[164:171], v216\n\t"
      "s_waitcnt lgkmcnt(0)\n\t"
      "v_mfma_f32_16x16x32_bf16 a[0:3], v[100:103], v[168:171], a[0:3]\n\t"
      "v_cvt_pk_bf16_f32 v179, v154, v155\n\t"
      "v_mfma_f32_16x16x32_bf16 a[0:3], v[76:79], v[164:167], a[0:3]\n\t"
      "v_exp_f32 v126, v126\n\t"
      "v_mfma_f32_16x16x32_bf16 a[4:7], v[80:83], v[164:167], a[4:7]\n\t"
      "v_exp_f32 v127, v127\n\t"
      "v_mfma_f32_16x16x32_bf16 a[4:7], v[104:107], v[168:171], a[4:7]\n\t"
      "v_exp_f32 v128, v128\n\t"
      "v_accvgpr_read_b32 v249, a228\n\t"
      "v_accvgpr_read_b32 v250, a229\n\t"
      "v_add_f32 v249, v249, v250\n\t"
      "ds_write_b32 v244, v249 offset:0\n\t"
      "v_mfma_f32_16x16x32_bf16 a[8:11], v[108:111], v[168:171], a[8:11]\n\t"
      "ds_write_b128 v240, a[0:3] offset:0\n\t"
      "v_mfma_f32_16x16x32_bf16 a[8:11], v[84:87], v[164:167], a[8:11]\n\t"
      "v_exp_f32 v129, v129\n\t"
      "v_mfma_f32_16x16x32_bf16 a[12:15], v[88:91], v[164:167], a[12:15]\n\t"
      "v_exp_f32 v130, v130\n\t"
      "v_mfma_f32_16x16x32_bf16 a[12:15], v[112:115], v[168:171], a[12:15]\n\t"
      "v_exp_f32 v131, v131\n\t"
      "ds_write_b128 v240, a[4:7] offset:1024\n\t"
      "v_mfma_f32_16x16x32_bf16 a[16:19], v[116:119], v[168:171], a[16:19]\n\t"
      "v_mfma_f32_16x16x32_bf16 a[16:19], v[92:95], v[164:167], a[16:19]\n\t"
      "v_exp_f32 v132, v132\n\t"
      "v_mfma_f32_16x16x32_bf16 a[20:23], v[96:99], v[164:167], a[20:23]\n\t"
      "v_exp_f32 v133, v133\n\t"
      "ds_write_b128 v240, a[8:11] offset:2048\n\t"
      "v_mfma_f32_16x16x32_bf16 a[20:23], v[120:123], v[168:171], a[20:23]\n\t"
      "v_cvt_pk_bf16_f32 v164, v124, v125\n\t"
      "v_cvt_pk_bf16_f32 v165, v126, v127\n\t"
      "v_mfma_f32_16x16x32_bf16 v[140:143], a[180:183], a[144:147], v[200:203]\n\t"
      "v_cvt_pk_bf16_f32 v166, v128, v129\n\t"
      "v_mfma_f32_16x16x32_bf16 v[140:143], a[184:187], a[148:151], v[140:143]\n\t"
      "ds_write_b128 v240, a[12:15] offset:3072\n\t"
      "v_mfma_f32_16x16x32_bf16 v[140:143], a[188:191], a[152:155], v[140:143]\n\t"
      "v_exp_f32 v134, v134\n\t"
      "v_mfma_f32_16x16x32_bf16 v[144:147], a[200:203], a[152:155], v[200:203]\n\t"
      "v_exp_f32 v135, v135\n\t"
      "v_mfma_f32_16x16x32_bf16 v[144:147], a[196:199], a[148:151], v[144:147]\n\t"
      "ds_write_b128 v240, a[16:19] offset:4096\n\t"
      "v_mfma_f32_16x16x32_bf16 v[144:147], a[192:195], a[144:147], v[144:147]\n\t"
      "v_exp_f32 v136, v136\n\t"
      "v_mfma_f32_16x16x32_bf16 v[148:151], a[204:207], a[144:147], v[200:203]\n\t"
      "v_exp_f32 v137, v137\n\t"
      "v_mfma_f32_16x16x32_bf16 v[148:151], a[208:211], a[148:151], v[148:151]\n\t"
      "v_cvt_pk_bf16_f32 v167, v130, v131\n\t"
      "ds_write_b128 v240, a[20:23] offset:5120\n\t"
      "v_mfma_f32_16x16x32_bf16 v[148:151], a[212:215], a[152:155], v[148:151]\n\t"
      "v_exp_f32 v138, v138\n\t"
      "v_mfma_f32_16x16x32_bf16 v[152:155], a[224:227], a[152:155], v[200:203]\n\t"
      "v_exp_f32 v139, v139\n\t"
      "v_mfma_f32_16x16x32_bf16 v[152:155], a[220:223], a[148:151], v[152:155]\n\t"
      "v_cvt_pk_bf16_f32 v168, v132, v133\n\t"
      "v_cvt_pk_bf16_f32 v169, v134, v135\n\t"
      "v_mfma_f32_16x16x32_bf16 v[152:155], a[216:219], a[144:147], v[152:155]\n\t"
      "v_exp_f32 v140, v140\n\t"
      "v_smfmac_f32_16x16x64_bf16 a[240:243], v[212:215], v[172:179], v216\n\t"
      "v_exp_f32 v141, v141\n\t"
      "v_mfma_f32_16x16x32_bf16 v[4:7], v[100:103], v[176:179], v[4:7]\n\t"
      "v_exp_f32 v142, v142\n\t"
      "s_waitcnt lgkmcnt(0)\n\t"
      "s_barrier\n\t"
      "ds_read_b32 v221, v245 offset:0\n\t"
      "ds_read_b128 v[184:187], v241 offset:0\n\t"
      "v_mfma_f32_16x16x32_bf16 v[4:7], v[76:79], v[172:175], v[4:7]\n\t"
      "v_exp_f32 v143, v143\n\t"
      "ds_read_b128 v[196:199], v241 offset:1024\n\t"
      "ds_read_b128 v[224:227], v241 offset:2048\n\t"
      "ds_read_b128 v[228:231], v241 offset:3072\n\t"
      "ds_read_b128 v[232:235], v241 offset:4096\n\t"
      "v_mfma_f32_16x16x32_bf16 v[8:11], v[80:83], v[172:175], v[8:11]\n\t"
      "v_exp_f32 v144, v144\n\t"
      "ds_read_b128 v[236:239], v241 offset:5120\n\t"
      "v_accvgpr_read_b32 v219, a240\n\t"
      "v_accvgpr_read_b32 v220, a241\n\t"
      "v_add_f32 v219, v219, v220\n\t"
      "v_mfma_f32_16x16x32_bf16 v[8:11], v[104:107], v[176:179], v[8:11]\n\t"
      "v_exp_f32 v145, v145\n\t"
      "ds_bpermute_b32 v218, v247, v219\n\t"
      "v_mfma_f32_16x16x32_bf16 v[12:15], v[108:111], v[176:179], v[12:15]\n\t"
      "v_exp_f32 v146, v146\n\t"
      "v_mfma_f32_16x16x32_bf16 v[12:15], v[84:87], v[172:175], v[12:15]\n\t"
      "v_exp_f32 v147, v147\n\t"
      "v_mfma_f32_16x16x32_bf16 v[16:19], v[88:91], v[172:175], v[16:19]\n\t"
      "v_exp_f32 v148, v148\n\t"
      "s_waitcnt lgkmcnt(5)\n\t"
      "v_pk_add_f32 v[4:5], v[4:5], v[184:185]\n\t"
      "v_pk_add_f32 v[6:7], v[6:7], v[186:187]\n\t"
      "v_mfma_f32_16x16x32_bf16 v[16:19], v[112:115], v[176:179], v[16:19]\n\t"
      "v_exp_f32 v149, v149\n\t"
      "v_pk_add_f32 v[8:9], v[8:9], v[196:197]\n\t"
      "v_pk_add_f32 v[10:11], v[10:11], v[198:199]\n\t"
      "v_mfma_f32_16x16x32_bf16 v[20:23], v[116:119], v[176:179], v[20:23]\n\t"
      "v_exp_f32 v150, v150\n\t"
      "s_waitcnt lgkmcnt(0)\n\t"
      "v_add_f32 v221, v221, v218\n\t"
      "v_subrev_u32 v217, 0.5, v221\n\t"
      "v_rcp_f32 v221, v221\n\t"
      "v_mfma_f32_16x16x32_bf16 v[20:23], v[92:95], v[172:175], v[20:23]\n\t"
      "v_exp_f32 v151, v151\n\t"
      "v_pk_mul_f32 v[4:5], v[4:5], v[220:221] op_sel:[0,1] op_sel_hi:[1,1]\n\t"
      "v_pk_mul_f32 v[6:7], v[6:7], v[220:221] op_sel:[0,1] op_sel_hi:[1,1]\n\t"
      "v_mfma_f32_16x16x32_bf16 v[24:27], v[96:99], v[172:175], v[24:27]\n\t"
      "v_exp_f32 v152, v152\n\t"
      "v_pk_mul_f32 v[8:9], v[8:9], v[220:221] op_sel:[0,1] op_sel_hi:[1,1]\n\t"
      "v_pk_mul_f32 v[10:11], v[10:11], v[220:221] op_sel:[0,1] op_sel_hi:[1,1]\n\t"
      "v_mfma_f32_16x16x32_bf16 v[24:27], v[120:123], v[176:179], v[24:27]\n\t"
      "v_cvt_pk_bf16_f32 v170, v136, v137\n\t"
      "v_cvt_pk_bf16_f32 v171, v138, v139\n\t"
      "v_cvt_pk_bf16_f32 v4, v4, v5\n\t"
      "v_cvt_pk_bf16_f32 v5, v6, v7\n\t"
      "v_cvt_pk_bf16_f32 v6, v8, v9\n\t"
      "v_cvt_pk_bf16_f32 v7, v10, v11\n\t"
      "v_mfma_f32_16x16x32_bf16 v[124:127], a[180:183], a[120:123], v[192:195]\n\t"
      "v_cvt_pk_bf16_f32 v172, v140, v141\n\t"
      "v_cvt_pk_bf16_f32 v173, v142, v143\n\t"
      "s_waitcnt lgkmcnt(3)\n\t"
      "v_pk_add_f32 v[12:13], v[12:13], v[224:225]\n\t"
      "v_pk_add_f32 v[14:15], v[14:15], v[226:227]\n\t"
      "v_mfma_f32_16x16x32_bf16 v[124:127], a[184:187], a[124:127], v[124:127]\n\t"
      "v_cvt_pk_bf16_f32 v174, v144, v145\n\t"
      "v_cvt_pk_bf16_f32 v175, v146, v147\n\t"
      "v_pk_add_f32 v[16:17], v[16:17], v[228:229]\n\t"
      "v_pk_add_f32 v[18:19], v[18:19], v[230:231]\n\t"
      "v_mfma_f32_16x16x32_bf16 v[124:127], a[188:191], a[128:131], v[124:127]\n\t"
      "v_exp_f32 v153, v153\n\t"
      "v_pk_mul_f32 v[12:13], v[12:13], v[220:221] op_sel:[0,1] op_sel_hi:[1,1]\n\t"
      "v_pk_mul_f32 v[14:15], v[14:15], v[220:221] op_sel:[0,1] op_sel_hi:[1,1]\n\t"
      "v_mfma_f32_16x16x32_bf16 v[128:131], a[200:203], a[128:131], v[192:195]\n\t"
      "v_exp_f32 v154, v154\n\t"
      "v_pk_mul_f32 v[16:17], v[16:17], v[220:221] op_sel:[0,1] op_sel_hi:[1,1]\n\t"
      "v_pk_mul_f32 v[18:19], v[18:19], v[220:221] op_sel:[0,1] op_sel_hi:[1,1]\n\t"
      "v_mfma_f32_16x16x32_bf16 v[128:131], a[196:199], a[124:127], v[128:131]\n\t"
      "v_exp_f32 v155, v155\n\t"
      "v_cvt_pk_bf16_f32 v12, v12, v13\n\t"
      "v_cvt_pk_bf16_f32 v13, v14, v15\n\t"
      "v_cvt_pk_bf16_f32 v14, v16, v17\n\t"
      "v_cvt_pk_bf16_f32 v15, v18, v19\n\t"
      "v_mfma_f32_16x16x32_bf16 v[128:131], a[192:195], a[120:123], v[128:131]\n\t"
      "v_cvt_pk_bf16_f32 v176, v148, v149\n\t"
      "v_cvt_pk_bf16_f32 v177, v150, v151\n\t"
      "s_mov_b64 vcc, s[36:37]\n\t"
      "v_cndmask_b32_dpp v8, v12, v4, vcc quad_perm:[1,0,3,2] row_mask:0xf bank_mask:0xf\n\t"
      "v_cndmask_b32_dpp v9, v13, v5, vcc quad_perm:[1,0,3,2] row_mask:0xf bank_mask:0xf\n\t"
      "v_cndmask_b32_dpp v10, v14, v6, vcc quad_perm:[1,0,3,2] row_mask:0xf bank_mask:0xf\n\t"
      "v_mfma_f32_16x16x32_bf16 v[132:135], a[204:207], a[120:123], v[192:195]\n\t"
      "v_exp_f32 v124, v124\n\t"
      "v_cndmask_b32_dpp v11, v15, v7, vcc quad_perm:[1,0,3,2] row_mask:0xf bank_mask:0xf\n\t"
      "global_store_dwordx4 %1, v[8:11], s[22:23] offset:0 sc1\n\t"
      "v_mfma_f32_16x16x32_bf16 v[132:135], a[208:211], a[124:127], v[132:135]\n\t"
      "v_exp_f32 v125, v125\n\t"
      "s_waitcnt lgkmcnt(1)\n\t"
      "v_pk_add_f32 v[20:21], v[20:21], v[232:233]\n\t"
      "v_pk_add_f32 v[22:23], v[22:23], v[234:235]\n\t"
      "v_mfma_f32_16x16x32_bf16 v[132:135], a[212:215], a[128:131], v[132:135]\n\t"
      "v_exp_f32 v126, v126\n\t"
      "v_pk_add_f32 v[24:25], v[24:25], v[236:237]\n\t"
      "v_pk_add_f32 v[26:27], v[26:27], v[238:239]\n\t"
      "v_mfma_f32_16x16x32_bf16 v[136:139], a[224:227], a[128:131], v[192:195]\n\t"
      "v_exp_f32 v127, v127\n\t"
      "v_pk_mul_f32 v[20:21], v[20:21], v[220:221] op_sel:[0,1] op_sel_hi:[1,1]\n\t"
      "v_pk_mul_f32 v[22:23], v[22:23], v[220:221] op_sel:[0,1] op_sel_hi:[1,1]\n\t"
      "v_mfma_f32_16x16x32_bf16 v[136:139], a[220:223], a[124:127], v[136:139]\n\t"
      "v_exp_f32 v128, v128\n\t"
      "v_pk_mul_f32 v[24:25], v[24:25], v[220:221] op_sel:[0,1] op_sel_hi:[1,1]\n\t"
      "v_pk_mul_f32 v[26:27], v[26:27], v[220:221] op_sel:[0,1] op_sel_hi:[1,1]\n\t"
      "v_mfma_f32_16x16x32_bf16 v[136:139], a[216:219], a[120:123], v[136:139]\n\t"
      "v_exp_f32 v129, v129\n\t"
      "v_cvt_pk_bf16_f32 v20, v20, v21\n\t"
      "v_cvt_pk_bf16_f32 v21, v22, v23\n\t"
      "v_cvt_pk_bf16_f32 v22, v24, v25\n\t"
      "v_cvt_pk_bf16_f32 v23, v26, v27\n\t"
      "v_smfmac_f32_16x16x64_bf16 a[232:235], v[212:215], v[164:171], v216\n\t"
      "v_exp_f32 v130, v130\n\t"
      "v_cndmask_b32 v16, v4, v20, vcc\n\t"
      "v_cndmask_b32 v17, v5, v21, vcc\n\t"
      "v_cndmask_b32 v18, v6, v22, vcc\n\t"
      "v_cndmask_b32 v19, v7, v23, vcc\n\t"
      "v_mfma_f32_16x16x32_bf16 a[24:27], v[100:103], v[168:171], a[24:27]\n\t"
      "v_exp_f32 v131, v131\n\t"
      "global_store_dwordx4 %1, v[16:19], s[22:23] offset:128 sc1\n\t"
      "v_mfma_f32_16x16x32_bf16 a[24:27], v[76:79], v[164:167], a[24:27]\n\t"
      "v_exp_f32 v132, v132\n\t"
      "v_accvgpr_read_b32 v249, a232\n\t"
      "v_accvgpr_read_b32 v250, a233\n\t"
      "v_add_f32 v249, v249, v250\n\t"
      "ds_write_b32 v244, v249 offset:64\n\t"
      "v_mfma_f32_16x16x32_bf16 a[28:31], v[80:83], v[164:167], a[28:31]\n\t"
      "v_exp_f32 v133, v133\n\t"
      "s_not_b64 vcc, vcc\n\t"
      "ds_write_b128 v240, a[24:27] offset:6144\n\t"
      "v_mfma_f32_16x16x32_bf16 a[28:31], v[104:107], v[168:171], a[28:31]\n\t"
      "v_exp_f32 v134, v134\n\t"
      "v_cndmask_b32_dpp v24, v12, v20, vcc quad_perm:[1,0,3,2] row_mask:0xf bank_mask:0xf\n\t"
      "v_cndmask_b32_dpp v25, v13, v21, vcc quad_perm:[1,0,3,2] row_mask:0xf bank_mask:0xf\n\t"
      "v_cndmask_b32_dpp v26, v14, v22, vcc quad_perm:[1,0,3,2] row_mask:0xf bank_mask:0xf\n\t"
      "v_cndmask_b32_dpp v27, v15, v23, vcc quad_perm:[1,0,3,2] row_mask:0xf bank_mask:0xf\n\t"
      "v_mfma_f32_16x16x32_bf16 a[32:35], v[108:111], v[168:171], a[32:35]\n\t"
      "v_exp_f32 v135, v135\n\t"
      "global_store_dwordx4 %1, v[24:27], s[22:23] offset:256 sc1\n\t"
      "v_mfma_f32_16x16x32_bf16 a[32:35], v[84:87], v[164:167], a[32:35]\n\t"
      "v_exp_f32 v136, v136\n\t"
      "ds_write_b128 v240, a[28:31] offset:7168\n\t"
      "v_mfma_f32_16x16x32_bf16 a[36:39], v[88:91], v[164:167], a[36:39]\n\t"
      "v_exp_f32 v137, v137\n\t"
      "v_mfma_f32_16x16x32_bf16 a[36:39], v[112:115], v[168:171], a[36:39]\n\t"
      "v_exp_f32 v138, v138\n\t"
      "v_mfma_f32_16x16x32_bf16 a[40:43], v[116:119], v[168:171], a[40:43]\n\t"
      "v_exp_f32 v139, v139\n\t"
      "ds_write_b128 v240, a[32:35] offset:8192\n\t"
      "v_mfma_f32_16x16x32_bf16 a[40:43], v[92:95], v[164:167], a[40:43]\n\t"
      "v_cvt_pk_bf16_f32 v178, v152, v153\n\t"
      "v_cvt_pk_bf16_f32 v179, v154, v155\n\t"
      "v_mfma_f32_16x16x32_bf16 a[44:47], v[96:99], v[164:167], a[44:47]\n\t"
      "v_mfma_f32_16x16x32_bf16 a[44:47], v[120:123], v[168:171], a[44:47]\n\t"
      "v_cvt_pk_bf16_f32 v164, v124, v125\n\t"
      "v_cvt_pk_bf16_f32 v165, v126, v127\n\t"
      "ds_write_b128 v240, a[36:39] offset:9216\n\t"
      "v_mfma_f32_16x16x32_bf16 v[140:143], a[180:183], a[156:159], v[204:207]\n\t"
      "v_cvt_pk_bf16_f32 v166, v128, v129\n\t"
      "v_cvt_pk_bf16_f32 v167, v130, v131\n\t"
      "v_mfma_f32_16x16x32_bf16 v[140:143], a[184:187], a[160:163], v[140:143]\n\t"
      "v_cvt_pk_bf16_f32 v168, v132, v133\n\t"
      "v_cvt_pk_bf16_f32 v169, v134, v135\n\t"
      "v_mfma_f32_16x16x32_bf16 v[140:143], a[188:191], a[164:167], v[140:143]\n\t"
      "ds_write_b128 v240, a[40:43] offset:10240\n\t"
      "v_mfma_f32_16x16x32_bf16 v[144:147], a[200:203], a[164:167], v[204:207]\n\t"
      "v_mfma_f32_16x16x32_bf16 v[144:147], a[196:199], a[160:163], v[144:147]\n\t"
      "v_cvt_pk_bf16_f32 v170, v136, v137\n\t"
      "v_cvt_pk_bf16_f32 v171, v138, v139\n\t"
      "v_mfma_f32_16x16x32_bf16 v[144:147], a[192:195], a[156:159], v[144:147]\n\t"
      "ds_write_b128 v240, a[44:47] offset:11264\n\t"
      "v_mfma_f32_16x16x32_bf16 v[148:151], a[204:207], a[156:159], v[204:207]\n\t"
      "v_exp_f32 v140, v140\n\t"
      "v_mfma_f32_16x16x32_bf16 v[148:151], a[208:211], a[160:163], v[148:151]\n\t"
      "v_exp_f32 v141, v141\n\t"
      "v_mfma_f32_16x16x32_bf16 v[148:151], a[212:215], a[164:167], v[148:151]\n\t"
      "v_mfma_f32_16x16x32_bf16 v[152:155], a[224:227], a[164:167], v[204:207]\n\t"
      "v_exp_f32 v142, v142\n\t"
      "v_mfma_f32_16x16x32_bf16 v[152:155], a[220:223], a[160:163], v[152:155]\n\t"
      "v_exp_f32 v143, v143\n\t"
      "v_mfma_f32_16x16x32_bf16 v[152:155], a[216:219], a[156:159], v[152:155]\n\t"
      "v_smfmac_f32_16x16x64_bf16 a[244:247], v[212:215], v[172:179], v216\n\t"
      "v_exp_f32 v144, v144\n\t"
      "s_waitcnt lgkmcnt(0)\n\t"
      "s_barrier\n\t"
      "ds_read_b32 v222, v245 offset:64\n\t"
      "ds_read_b128 v[184:187], v241 offset:6144\n\t"
      "v_mfma_f32_16x16x32_bf16 v[28:31], v[100:103], v[176:179], v[28:31]\n\t"
      "ds_read_b128 v[196:199], v241 offset:7168\n\t"
      "ds_read_b128 v[224:227], v241 offset:8192\n\t"
      "ds_read_b128 v[228:231], v241 offset:9216\n\t"
      "ds_read_b128 v[232:235], v241 offset:10240\n\t"
      "ds_read_b128 v[236:239], v241 offset:11264\n\t"
      "v_accvgpr_read_b32 v219, a244\n\t"
      "v_mfma_f32_16x16x32_bf16 v[28:31], v[76:79], v[172:175], v[28:31]\n\t"
      "v_exp_f32 v145, v145\n\t"
      "v_accvgpr_read_b32 v220, a245\n\t"
      "v_add_f32 v219, v219, v220\n\t"
      "ds_bpermute_b32 v218, v247, v219\n\t"
      "v_mfma_f32_16x16x32_bf16 v[32:35], v[80:83], v[172:175], v[32:35]\n\t"
      "v_mfma_f32_16x16x32_bf16 v[32:35], v[104:107], v[176:179], v[32:35]\n\t"
      "v_exp_f32 v146, v146\n\t"
      "v_mfma_f32_16x16x32_bf16 v[36:39], v[108:111], v[176:179], v[36:39]\n\t"
      "v_exp_f32 v147, v147\n\t"
      "v_mfma_f32_16x16x32_bf16 v[36:39], v[84:87], v[172:175], v[36:39]\n\t"
      "v_exp_f32 v148, v148\n\t"
      "s_waitcnt lgkmcnt(5)\n\t"
      "v_pk_add_f32 v[28:29], v[28:29], v[184:185]\n\t"
      "v_pk_add_f32 v[30:31], v[30:31], v[186:187]\n\t"
      "v_mfma_f32_16x16x32_bf16 v[40:43], v[88:91], v[172:175], v[40:43]\n\t"
      "v_exp_f32 v149, v149\n\t"
      "v_pk_add_f32 v[32:33], v[32:33], v[196:197]\n\t"
      "v_pk_add_f32 v[34:35], v[34:35], v[198:199]\n\t"
      "v_mfma_f32_16x16x32_bf16 v[40:43], v[112:115], v[176:179], v[40:43]\n\t"
      "v_exp_f32 v150, v150\n\t"
      "s_waitcnt lgkmcnt(0)\n\t"
      "v_add_f32 v222, v222, v218\n\t"
      "v_subrev_u32 v220, 0.5, v222\n\t"
      "v_max_u32 v217, v217, v220\n\t"
      "v_mfma_f32_16x16x32_bf16 v[44:47], v[116:119], v[176:179], v[44:47]\n\t"
      "v_rcp_f32 v222, v222\n\t"
      "s_nop 0\n\t"
      "v_pk_mul_f32 v[28:29], v[28:29], v[222:223] op_sel_hi:[1,0]\n\t"
      "v_pk_mul_f32 v[30:31], v[30:31], v[222:223] op_sel_hi:[1,0]\n\t"
      "v_mfma_f32_16x16x32_bf16 v[44:47], v[92:95], v[172:175], v[44:47]\n\t"
      "v_pk_mul_f32 v[32:33], v[32:33], v[222:223] op_sel_hi:[1,0]\n\t"
      "v_pk_mul_f32 v[34:35], v[34:35], v[222:223] op_sel_hi:[1,0]\n\t"
      "v_cvt_pk_bf16_f32 v28, v28, v29\n\t"
      "v_cvt_pk_bf16_f32 v29, v30, v31\n\t"
      "v_mfma_f32_16x16x32_bf16 v[48:51], v[96:99], v[172:175], v[48:51]\n\t"
      "v_exp_f32 v151, v151\n\t"
      "v_cvt_pk_bf16_f32 v30, v32, v33\n\t"
      "v_cvt_pk_bf16_f32 v31, v34, v35\n\t"
      "s_waitcnt lgkmcnt(3)\n\t"
      "v_pk_add_f32 v[36:37], v[36:37], v[224:225]\n\t"
      "v_mfma_f32_16x16x32_bf16 v[48:51], v[120:123], v[176:179], v[48:51]\n\t"
      "v_cvt_pk_bf16_f32 v172, v140, v141\n\t"
      "v_cvt_pk_bf16_f32 v173, v142, v143\n\t"
      "v_cvt_pk_bf16_f32 v174, v144, v145\n\t"
      "v_cvt_pk_bf16_f32 v175, v146, v147\n\t"
      "v_exp_f32 v152, v152\n\t"
      "v_exp_f32 v153, v153\n\t"
      "v_exp_f32 v154, v154\n\t"
      "v_cvt_pk_bf16_f32 v176, v148, v149\n\t"
      "v_cvt_pk_bf16_f32 v177, v150, v151\n\t"
      "v_exp_f32 v155, v155\n\t"
      "v_cvt_pk_bf16_f32 v178, v152, v153\n\t"
      "v_smfmac_f32_16x16x64_bf16 a[236:239], v[212:215], v[164:171], v216\n\t"
      "v_pk_add_f32 v[38:39], v[38:39], v[226:227]\n\t"
      "v_pk_add_f32 v[40:41], v[40:41], v[228:229]\n\t"
      "v_pk_add_f32 v[42:43], v[42:43], v[230:231]\n\t"
      "v_mfma_f32_16x16x32_bf16 a[48:51], v[100:103], v[168:171], a[48:51]\n\t"
      "v_pk_mul_f32 v[36:37], v[36:37], v[222:223] op_sel_hi:[1,0]\n\t"
      "v_pk_mul_f32 v[38:39], v[38:39], v[222:223] op_sel_hi:[1,0]\n\t"
      "v_pk_mul_f32 v[40:41], v[40:41], v[222:223] op_sel_hi:[1,0]\n\t"
      "v_mfma_f32_16x16x32_bf16 a[48:51], v[76:79], v[164:167], a[48:51]\n\t"
      "v_cvt_pk_bf16_f32 v179, v154, v155\n\t"
      "v_accvgpr_read_b32 v249, a236\n\t"
      "v_accvgpr_read_b32 v250, a237\n\t"
      "v_add_f32 v249, v249, v250\n\t"
      "ds_write_b32 v244, v249 offset:128\n\t"
      "v_pk_mul_f32 v[42:43], v[42:43], v[222:223] op_sel_hi:[1,0]\n\t"
      "v_mfma_f32_16x16x32_bf16 a[52:55], v[80:83], v[164:167], a[52:55]\n\t"
      "v_cvt_pk_bf16_f32 v36, v36, v37\n\t"
      "ds_write_b128 v240, a[48:51] offset:12288\n\t"
      "v_cvt_pk_bf16_f32 v37, v38, v39\n\t"
      "v_mfma_f32_16x16x32_bf16 a[52:55], v[104:107], v[168:171], a[52:55]\n\t"
      "v_cvt_pk_bf16_f32 v38, v40, v41\n\t"
      "v_cvt_pk_bf16_f32 v39, v42, v43\n\t"
      "s_mov_b64 vcc, s[36:37]\n\t"
      "v_cndmask_b32_dpp v32, v36, v28, vcc quad_perm:[1,0,3,2] row_mask:0xf bank_mask:0xf\n\t"
      "v_cndmask_b32_dpp v33, v37, v29, vcc quad_perm:[1,0,3,2] row_mask:0xf bank_mask:0xf\n\t"
      "v_cndmask_b32_dpp v34, v38, v30, vcc quad_perm:[1,0,3,2] row_mask:0xf bank_mask:0xf\n\t"
      "v_mfma_f32_16x16x32_bf16 a[56:59], v[108:111], v[168:171], a[56:59]\n\t"
      "v_cndmask_b32_dpp v35, v39, v31, vcc quad_perm:[1,0,3,2] row_mask:0xf bank_mask:0xf\n\t"
      "global_store_dwordx4 %2, v[32:35], s[22:23] offset:0 sc1\n\t"
      "ds_write_b128 v240, a[52:55] offset:13312\n\t"
      "v_mfma_f32_16x16x32_bf16 a[56:59], v[84:87], v[164:167], a[56:59]\n\t"
      "s_waitcnt lgkmcnt(4)\n\t"
      "v_pk_add_f32 v[44:45], v[44:45], v[232:233]\n\t"
      "v_pk_add_f32 v[46:47], v[46:47], v[234:235]\n\t"
      "v_pk_add_f32 v[48:49], v[48:49], v[236:237]\n\t"
      "v_mfma_f32_16x16x32_bf16 a[60:63], v[88:91], v[164:167], a[60:63]\n\t"
      "v_pk_add_f32 v[50:51], v[50:51], v[238:239]\n\t"
      "v_pk_mul_f32 v[44:45], v[44:45], v[222:223] op_sel_hi:[1,0]\n\t"
      "v_pk_mul_f32 v[46:47], v[46:47], v[222:223] op_sel_hi:[1,0]\n\t"
      "v_mfma_f32_16x16x32_bf16 a[60:63], v[112:115], v[168:171], a[60:63]\n\t"
      "ds_write_b128 v240, a[56:59] offset:14336\n\t"
      "v_pk_mul_f32 v[48:49], v[48:49], v[222:223] op_sel_hi:[1,0]\n\t"
      "v_mfma_f32_16x16x32_bf16 a[64:67], v[116:119], v[168:171], a[64:67]\n\t"
      "v_pk_mul_f32 v[50:51], v[50:51], v[222:223] op_sel_hi:[1,0]\n\t"
      "v_cvt_pk_bf16_f32 v44, v44, v45\n\t"
      "v_cvt_pk_bf16_f32 v45, v46, v47\n\t"
      "v_cvt_pk_bf16_f32 v46, v48, v49\n\t"
      "v_cvt_pk_bf16_f32 v47, v50, v51\n\t"
      "v_mfma_f32_16x16x32_bf16 a[64:67], v[92:95], v[164:167], a[64:67]\n\t"
      "ds_write_b128 v240, a[60:63] offset:15360\n\t"
      "v_cndmask_b32 v40, v28, v44, vcc\n\t"
      "v_cndmask_b32 v41, v29, v45, vcc\n\t"
      "v_mfma_f32_16x16x32_bf16 a[68:71], v[96:99], v[164:167], a[68:71]\n\t"
      "v_cndmask_b32 v42, v30, v46, vcc\n\t"
      "v_cndmask_b32 v43, v31, v47, vcc\n\t"
      "global_store_dwordx4 %2, v[40:43], s[22:23] offset:128 sc1\n\t"
      "v_mfma_f32_16x16x32_bf16 a[68:71], v[120:123], v[168:171], a[68:71]\n\t"
      "ds_write_b128 v240, a[64:67] offset:16384\n\t"
      "s_not_b64 vcc, vcc\n\t"
      "v_cndmask_b32_dpp v48, v36, v44, vcc quad_perm:[1,0,3,2] row_mask:0xf bank_mask:0xf\n\t"
      "v_smfmac_f32_16x16x64_bf16 a[248:251], v[212:215], v[172:179], v216\n\t"
      "v_cndmask_b32_dpp v49, v37, v45, vcc quad_perm:[1,0,3,2] row_mask:0xf bank_mask:0xf\n\t"
      "v_cndmask_b32_dpp v50, v38, v46, vcc quad_perm:[1,0,3,2] row_mask:0xf bank_mask:0xf\n\t"
      "v_cndmask_b32_dpp v51, v39, v47, vcc quad_perm:[1,0,3,2] row_mask:0xf bank_mask:0xf\n\t"
      "global_store_dwordx4 %2, v[48:51], s[22:23] offset:256 sc1\n\t"
      "v_mfma_f32_16x16x32_bf16 v[52:55], v[100:103], v[176:179], v[52:55]\n\t"
      "ds_write_b128 v240, a[68:71] offset:17408\n\t"
      "v_mfma_f32_16x16x32_bf16 v[52:55], v[76:79], v[172:175], v[52:55]\n\t"
      "v_mfma_f32_16x16x32_bf16 v[56:59], v[80:83], v[172:175], v[56:59]\n\t"
      "v_mfma_f32_16x16x32_bf16 v[56:59], v[104:107], v[176:179], v[56:59]\n\t"
      "v_mfma_f32_16x16x32_bf16 v[60:63], v[108:111], v[176:179], v[60:63]\n\t"
      "v_mfma_f32_16x16x32_bf16 v[60:63], v[84:87], v[172:175], v[60:63]\n\t"
      "v_mfma_f32_16x16x32_bf16 v[64:67], v[88:91], v[172:175], v[64:67]\n\t"
      "v_mfma_f32_16x16x32_bf16 v[64:67], v[112:115], v[176:179], v[64:67]\n\t"
      "s_waitcnt lgkmcnt(0)\n\t"
      "s_barrier\n\t"
      "ds_read_b32 v221, v245 offset:128\n\t"
      "ds_read_b128 v[184:187], v241 offset:12288\n\t"
      "ds_read_b128 v[196:199], v241 offset:13312\n\t"
      "ds_read_b128 v[224:227], v241 offset:14336\n\t"
      "v_mfma_f32_16x16x32_bf16 v[68:71], v[116:119], v[176:179], v[68:71]\n\t"
      "ds_read_b128 v[228:231], v241 offset:15360\n\t"
      "ds_read_b128 v[232:235], v241 offset:16384\n\t"
      "ds_read_b128 v[236:239], v241 offset:17408\n\t"
      "v_accvgpr_read_b32 v219, a248\n\t"
      "v_accvgpr_read_b32 v220, a249\n\t"
      "v_add_f32 v219, v219, v220\n\t"
      "v_mfma_f32_16x16x32_bf16 v[68:71], v[92:95], v[172:175], v[68:71]\n\t"
      "ds_bpermute_b32 v218, v247, v219\n\t"
      "v_mfma_f32_16x16x32_bf16 v[72:75], v[96:99], v[172:175], v[72:75]\n\t"
      "v_mfma_f32_16x16x32_bf16 v[72:75], v[120:123], v[176:179], v[72:75]\n\t"
      "s_waitcnt lgkmcnt(5)\n\t"
      "v_pk_add_f32 v[52:53], v[52:53], v[184:185]\n\t"
      "v_pk_add_f32 v[54:55], v[54:55], v[186:187]\n\t"
      "v_pk_add_f32 v[56:57], v[56:57], v[196:197]\n\t"
      "v_pk_add_f32 v[58:59], v[58:59], v[198:199]\n\t"
      "s_waitcnt lgkmcnt(0)\n\t"
      "v_add_f32 v221, v221, v218\n\t"
      "v_subrev_u32 v220, 0.5, v221\n\t"
      "v_max_u32 v217, v217, v220\n\t"
      "v_rcp_f32 v221, v221\n\t"
      "s_nop 0\n\t"
      "v_pk_mul_f32 v[52:53], v[52:53], v[220:221] op_sel:[0,1] op_sel_hi:[1,1]\n\t"
      "v_pk_mul_f32 v[54:55], v[54:55], v[220:221] op_sel:[0,1] op_sel_hi:[1,1]\n\t"
      "v_pk_mul_f32 v[56:57], v[56:57], v[220:221] op_sel:[0,1] op_sel_hi:[1,1]\n\t"
      "v_pk_mul_f32 v[58:59], v[58:59], v[220:221] op_sel:[0,1] op_sel_hi:[1,1]\n\t"
      "v_cvt_pk_bf16_f32 v52, v52, v53\n\t"
      "v_cvt_pk_bf16_f32 v53, v54, v55\n\t"
      "v_cvt_pk_bf16_f32 v54, v56, v57\n\t"
      "v_cvt_pk_bf16_f32 v55, v58, v59\n\t"
      "s_waitcnt lgkmcnt(3)\n\t"
      "v_pk_add_f32 v[60:61], v[60:61], v[224:225]\n\t"
      "v_pk_add_f32 v[62:63], v[62:63], v[226:227]\n\t"
      "v_pk_add_f32 v[64:65], v[64:65], v[228:229]\n\t"
      "v_pk_add_f32 v[66:67], v[66:67], v[230:231]\n\t"
      "v_pk_mul_f32 v[60:61], v[60:61], v[220:221] op_sel:[0,1] op_sel_hi:[1,1]\n\t"
      "v_pk_mul_f32 v[62:63], v[62:63], v[220:221] op_sel:[0,1] op_sel_hi:[1,1]\n\t"
      "v_pk_mul_f32 v[64:65], v[64:65], v[220:221] op_sel:[0,1] op_sel_hi:[1,1]\n\t"
      "v_pk_mul_f32 v[66:67], v[66:67], v[220:221] op_sel:[0,1] op_sel_hi:[1,1]\n\t"
      "v_cvt_pk_bf16_f32 v60, v60, v61\n\t"
      "v_cvt_pk_bf16_f32 v61, v62, v63\n\t"
      "v_cvt_pk_bf16_f32 v62, v64, v65\n\t"
      "v_cvt_pk_bf16_f32 v63, v66, v67\n\t"
      "s_mov_b64 vcc, s[36:37]\n\t"
      "v_cndmask_b32_dpp v56, v60, v52, vcc quad_perm:[1,0,3,2] row_mask:0xf bank_mask:0xf\n\t"
      "v_cndmask_b32_dpp v57, v61, v53, vcc quad_perm:[1,0,3,2] row_mask:0xf bank_mask:0xf\n\t"
      "v_cndmask_b32_dpp v58, v62, v54, vcc quad_perm:[1,0,3,2] row_mask:0xf bank_mask:0xf\n\t"
      "v_cndmask_b32_dpp v59, v63, v55, vcc quad_perm:[1,0,3,2] row_mask:0xf bank_mask:0xf\n\t"
      "global_store_dwordx4 %3, v[56:59], s[22:23] offset:0 sc1\n\t"
      "s_waitcnt lgkmcnt(1)\n\t"
      "v_pk_add_f32 v[68:69], v[68:69], v[232:233]\n\t"
      "v_pk_add_f32 v[70:71], v[70:71], v[234:235]\n\t"
      "v_pk_add_f32 v[72:73], v[72:73], v[236:237]\n\t"
      "v_pk_add_f32 v[74:75], v[74:75], v[238:239]\n\t"
      "v_pk_mul_f32 v[68:69], v[68:69], v[220:221] op_sel:[0,1] op_sel_hi:[1,1]\n\t"
      "v_pk_mul_f32 v[70:71], v[70:71], v[220:221] op_sel:[0,1] op_sel_hi:[1,1]\n\t"
      "v_pk_mul_f32 v[72:73], v[72:73], v[220:221] op_sel:[0,1] op_sel_hi:[1,1]\n\t"
      "v_pk_mul_f32 v[74:75], v[74:75], v[220:221] op_sel:[0,1] op_sel_hi:[1,1]\n\t"
      "v_cvt_pk_bf16_f32 v68, v68, v69\n\t"
      "v_cvt_pk_bf16_f32 v69, v70, v71\n\t"
      "v_cvt_pk_bf16_f32 v70, v72, v73\n\t"
      "v_cvt_pk_bf16_f32 v71, v74, v75\n\t"
      "v_cndmask_b32 v64, v52, v68, vcc\n\t"
      "v_cndmask_b32 v65, v53, v69, vcc\n\t"
      "v_cndmask_b32 v66, v54, v70, vcc\n\t"
      "v_cndmask_b32 v67, v55, v71, vcc\n\t"
      "global_store_dwordx4 %3, v[64:67], s[22:23] offset:128 sc1\n\t"
      "s_not_b64 vcc, vcc\n\t"
      "v_cndmask_b32_dpp v72, v60, v68, vcc quad_perm:[1,0,3,2] row_mask:0xf bank_mask:0xf\n\t"
      "v_cndmask_b32_dpp v73, v61, v69, vcc quad_perm:[1,0,3,2] row_mask:0xf bank_mask:0xf\n\t"
      "v_cndmask_b32_dpp v74, v62, v70, vcc quad_perm:[1,0,3,2] row_mask:0xf bank_mask:0xf\n\t"
      "v_cndmask_b32_dpp v75, v63, v71, vcc quad_perm:[1,0,3,2] row_mask:0xf bank_mask:0xf\n\t"
      "global_store_dwordx4 %3, v[72:75], s[22:23] offset:256 sc1\n\t"
      "s_mov_b32 s48, 847249408\n\t"
      "s_waitcnt vmcnt(9)\n\t"
      "v_cmp_ne_u32 vcc, s54, v223\n\t"
      "v_cmp_gt_u32 s[52:53], s48, v217\n\t"
      "s_nop 0\n\t"
      "s_and_b64 s[52:53], s[52:53], vcc\n\t"
      "s_branch Lfinish%=\n\t"
      "LslowL%=:\n\t"
      "global_load_dword v222, v221, s[58:59] sc1\n\t"
      "s_add_u32 s57, s57, 1\n\t"
      "s_waitcnt vmcnt(0)\n\t"
      "v_cmp_ne_u32 vcc, s54, v222\n\t"
      "s_cmp_lt_u32 s57, 0x100000\n\t"
      "s_cbranch_scc0 LbackL%=\n\t"
      "s_cbranch_vccnz LslowL%=\n\t"
      "s_branch LbackL%=\n\t"
      "LslowH%=:\n\t"
      "global_load_dword v222, v221, s[58:59] sc1\n\t"
      "s_add_u32 s57, s57, 1\n\t"
      "s_waitcnt vmcnt(0)\n\t"
      "v_cmp_ne_u32 vcc, s54, v222\n\t"
      "s_cmp_lt_u32 s57, 0x100000\n\t"
      "s_cbranch_scc0 LbackH%=\n\t"
      "s_cbranch_vccnz LslowH%=\n\t"
      "s_branch LbackH%=\n\t"
      "Lheavy%=:\n\t"
      "ds_read_b128 a[180:183], v217 offset:0\n\t"
      "ds_read_b128 a[184:187], v217 offset:1024\n\t"
      "ds_read_b128 a[188:191], v217 offset:2048\n\t"
      "ds_read_b128 a[200:203], v217 offset:5120\n\t"
      "ds_read_b128 a[196:199], v217 offset:4096\n\t"
      "ds_read_b128 a[192:195], v217 offset:3072\n\t"
      "ds_read_b128 a[204:207], v218 offset:0\n\t"
      "ds_read_b128 a[208:211], v218 offset:1024\n\t"
      "ds_read_b128 a[212:215], v218 offset:2048\n\t"
      "ds_read_b128 a[224:227], v218 offset:5120\n\t"
      "ds_read_b128 a[220:223], v218 offset:4096\n\t"
      "ds_read_b128 a[216:219], v218 offset:3072\n\t"
      "s_xor_b32 s42, s42, 0x10000\n\t"
      "s_add_u32 s58, s58, 57344\n\t"
      "s_addc_u32 s59, s59, 0\n\t"
      "s_mov_b32 m0, s42\n\t"
      "s_xor_b32 s42, s42, 0x10000\n\t"
      "s_waitcnt lgkmcnt(9)\n\t"
      "v_mfma_f32_16x16x32_bf16 v[124:127], a[180:183], a[96:99], v[184:187]\n\t"
      "v_mfma_f32_16x16x32_bf16 v[124:127], a[184:187], a[100:103], v[124:127]\n\t"
      "v_mfma_f32_16x16x32_bf16 v[124:127], a[188:191], a[104:107], v[124:127]\n\t"
      "s_waitcnt lgkmcnt(6)\n\t"
      "v_mfma_f32_16x16x32_bf16 v[128:131], a[200:203], a[104:107], v[184:187]\n\t"
      "v_mfma_f32_16x16x32_bf16 v[128:131], a[196:199], a[100:103], v[128:131]\n\t"
      "v_mfma_f32_16x16x32_bf16 v[128:131], a[192:195], a[96:99], v[128:131]\n\t"
      "s_waitcnt lgkmcnt(3)\n\t"
      "v_mfma_f32_16x16x32_bf16 v[132:135], a[204:207], a[96:99], v[184:187]\n\t"
      "v_mfma_f32_16x16x32_bf16 v[132:135], a[208:211], a[100:103], v[132:135]\n\t"
      "v_mfma_f32_16x16x32_bf16 v[132:135], a[212:215], a[104:107], v[132:135]\n\t"
      "s_waitcnt lgkmcnt(0)\n\t"
      "v_mfma_f32_16x16x32_bf16 v[136:139], a[224:227], a[104:107], v[184:187]\n\t"
      "v_exp_f32 v124, v124\n\t"
      "v_mfma_f32_16x16x32_bf16 v[136:139], a[220:223], a[100:103], v[136:139]\n\t"
      "v_mfma_f32_16x16x32_bf16 v[136:139], a[216:219], a[96:99], v[136:139]\n\t"
      "Lp1Ha%=:\n\t"
      "s_waitcnt vmcnt(0)\n\t"
      "v_cmp_ne_u32 vcc, s54, v222\n\t"
      "s_add_u32 s57, s57, 1\n\t"
      "s_and_b32 s48, vcc_lo, 0xff00\n\t"
      "s_cbranch_scc0 Lp1Hb%=\n\t"
      "global_load_dword v222, v224, s[40:41] sc1\n\t"
      "s_cmp_lt_u32 s57, 0x100000\n\t"
      "s_cbranch_scc1 Lp1Ha%=\n\t"
      "Lp1Hb%=:\n\t"
      "s_barrier\n\t"
      "ds_read_b128 v[100:103], v218 offset:6144\n\t"
      "ds_read_b128 v[76:79], v217 offset:6144\n\t"
      "ds_read_b128 v[80:83], v217 offset:7168\n\t"
      "ds_read_b128 v[104:107], v218 offset:7168\n\t"
      "ds_read_b128 v[108:111], v218 offset:8192\n\t"
      "ds_read_b128 v[84:87], v217 offset:8192\n\t"
      "ds_read_b128 v[88:91], v217 offset:9216\n\t"
      "ds_read_b128 v[112:115], v218 offset:9216\n\t"
      "v_exp_f32 v125, v125\n\t"
      "v_exp_f32 v126, v126\n\t"
      "v_exp_f32 v127, v127\n\t"
      "v_exp_f32 v128, v128\n\t"
      "v_exp_f32 v129, v129\n\t"
      "v_exp_f32 v130, v130\n\t"
      "v_exp_f32 v131, v131\n\t"
      "v_exp_f32 v132, v132\n\t"
      "v_exp_f32 v133, v133\n\t"
      "v_exp_f32 v134, v134\n\t"
      "v_mfma_f32_16x16x32_bf16 v[140:143], a[180:183], a[132:135], v[196:199]\n\t"
      "global_load_lds_dwordx4 v219, s[58:59] offset:-3072 sc1\n\t"
      "v_cvt_pk_bf16_f32 v164, v124, v125\n\t"
      "v_cvt_pk_bf16_f32 v165, v126, v127\n\t"
      "v_mfma_f32_16x16x32_bf16 v[140:143], a[184:187], a[136:139], v[140:143]\n\t"
      "v_exp_f32 v135, v135\n\t"
      "v_mfma_f32_16x16x32_bf16 v[140:143], a[188:191], a[140:143], v[140:143]\n\t"
      "global_load_lds_dwordx4 v219, s[58:59] offset:-2048 sc1\n\t"
      "v_cvt_pk_bf16_f32 v166, v128, v129\n\t"
      "v_cvt_pk_bf16_f32 v167, v130, v131\n\t"
      "v_mfma_f32_16x16x32_bf16 v[144:147], a[200:203], a[140:143], v[196:199]\n\t"
      "v_exp_f32 v136, v136\n\t"
      "v_mfma_f32_16x16x32_bf16 v[144:147], a[196:199], a[136:139], v[144:147]\n\t"
      "global_load_lds_dwordx4 v219, s[58:59] offset:-1024 sc1\n\t"
      "v_exp_f32 v137, v137\n\t"
      "v_mfma_f32_16x16x32_bf16 v[144:147], a[192:195], a[132:135], v[144:147]\n\t"
      "v_exp_f32 v138, v138\n\t"
      "v_mfma_f32_16x16x32_bf16 v[148:151], a[204:207], a[132:135], v[196:199]\n\t"
      "global_load_lds_dwordx4 v219, s[58:59] offset:0 sc1\n\t"
      "v_exp_f32 v139, v139\n\t"
      "v_mfma_f32_16x16x32_bf16 v[148:151], a[208:211], a[136:139], v[148:151]\n\t"
      "v_cvt_pk_bf16_f32 v168, v132, v133\n\t"
      "v_cvt_pk_bf16_f32 v169, v134, v135\n\t"
      "v_mfma_f32_16x16x32_bf16 v[148:151], a[212:215], a[140:143], v[148:151]\n\t"
      "global_load_lds_dwordx4 v219, s[58:59] offset:1024 sc1\n\t"
      "v_exp_f32 v140, v140\n\t"
      "v_mfma_f32_16x16x32_bf16 v[152:155], a[224:227], a[140:143], v[196:199]\n\t"
      "v_exp_f32 v141, v141\n\t"
      "v_mfma_f32_16x16x32_bf16 v[152:155], a[220:223], a[136:139], v[152:155]\n\t"
      "global_load_lds_dwordx4 v219, s[58:59] offset:2048 sc1\n\t"
      "v_exp_f32 v142, v142\n\t"
      "v_mfma_f32_16x16x32_bf16 v[152:155], a[216:219], a[132:135], v[152:155]\n\t"
      "v_exp_f32 v143, v143\n\t"
      "v_exp_f32 v144, v144\n\t"
      "v_exp_f32 v145, v145\n\t"
      "v_exp_f32 v146, v146\n\t"
      "v_exp_f32 v147, v147\n\t"
      "v_exp_f32 v148, v148\n\t"
      "v_exp_f32 v149, v149\n\t"
      "v_exp_f32 v150, v150\n\t"
      "v_cvt_pk_bf16_f32 v170, v136, v137\n\t"
      "v_cvt_pk_bf16_f32 v171, v138, v139\n\t"
      "s_mov_b64 exec, 1\n\t"
      "global_load_dword v239, %1, s[22:23]\n\t"
      "s_mov_b64 exec, -1\n\t"
      ".p2align 6\n\t"
      "Lmain_loopH%=:\n\t"
      "v_mfma_f32_16x16x32_bf16 v[156:159], a[180:183], a[168:171], v[208:211]\n\t"
      "ds_read_b128 v[116:119], v218 offset:10240\n\t"
      "ds_read_b128 v[92:95], v217 offset:10240\n\t"
      "v_mfma_f32_16x16x32_bf16 v[156:159], a[184:187], a[172:175], v[156:159]\n\t"
      "v_cvt_pk_bf16_f32 v172, v140, v141\n\t"
      "v_cvt_pk_bf16_f32 v173, v142, v143\n\t"
      "v_mfma_f32_16x16x32_bf16 v[156:159], a[188:191], a[176:179], v[156:159]\n\t"
      "ds_read_b128 v[96:99], v217 offset:11264\n\t"
      "ds_read_b128 v[120:123], v218 offset:11264\n\t"
      "v_mfma_f32_16x16x32_bf16 v[160:163], a[200:203], a[176:179], v[208:211]\n\t"
      "v_exp_f32 v151, v151\n\t"
      "v_mfma_f32_16x16x32_bf16 v[160:163], a[196:199], a[172:175], v[160:163]\n\t"
      "s_add_u32 m0, m0, 6144\n\t"
      "v_cvt_pk_bf16_f32 v174, v144, v145\n\t"
      "v_mfma_f32_16x16x32_bf16 v[160:163], a[192:195], a[168:171], v[160:163]\n\t"
      "global_load_lds_dwordx4 v220, s[58:59] offset:-3072 sc1\n\t"
      "v_smfmac_f32_16x16x64_bf16 a[228:231], v[212:215], v[164:171], v216\n\t"
      "v_xor_b32 v217, 0x10000, v217\n\t"
      "s_waitcnt lgkmcnt(0)\n\t"
      "v_mfma_f32_16x16x32_bf16 a[0:3], v[100:103], v[168:171], a[0:3]\n\t"
      "v_exp_f32 v152, v152\n\t"
      "v_mfma_f32_16x16x32_bf16 a[0:3], v[76:79], v[164:167], a[0:3]\n\t"
      "v_exp_f32 v153, v153\n\t"
      "v_mfma_f32_16x16x32_bf16 a[4:7], v[80:83], v[164:167], a[4:7]\n\t"
      "global_load_lds_dwordx4 v220, s[58:59] offset:-2048 sc1\n\t"
      "v_mfma_f32_16x16x32_bf16 a[4:7], v[104:107], v[168:171], a[4:7]\n\t"
      "v_cvt_pk_bf16_f32 v175, v146, v147\n\t"
      "v_cvt_pk_bf16_f32 v176, v148, v149\n\t"
      "v_mfma_f32_16x16x32_bf16 a[8:11], v[108:111], v[168:171], a[8:11]\n\t"
      "v_exp_f32 v154, v154\n\t"
      "v_mfma_f32_16x16x32_bf16 a[8:11], v[84:87], v[164:167], a[8:11]\n\t"
      "v_exp_f32 v155, v155\n\t"
      "v_mfma_f32_16x16x32_bf16 a[12:15], v[88:91], v[164:167], a[12:15]\n\t"
      "global_load_lds_dwordx4 v220, s[58:59] offset:-1024 sc1\n\t"
      "v_mfma_f32_16x16x32_bf16 a[12:15], v[112:115], v[168:171], a[12:15]\n\t"
      "v_exp_f32 v156, v156\n\t"
      "v_mfma_f32_16x16x32_bf16 a[16:19], v[116:119], v[168:171], a[16:19]\n\t"
      "v_exp_f32 v157, v157\n\t"
      "v_mfma_f32_16x16x32_bf16 a[16:19], v[92:95], v[164:167], a[16:19]\n\t"
      "v_cvt_pk_bf16_f32 v177, v150, v151\n\t"
      "v_cvt_pk_bf16_f32 v178, v152, v153\n\t"
      "v_mfma_f32_16x16x32_bf16 a[20:23], v[96:99], v[164:167], a[20:23]\n\t"
      "global_load_lds_dwordx4 v220, s[58:59] offset:0 sc1\n\t"
      "v_mfma_f32_16x16x32_bf16 a[20:23], v[120:123], v[168:171], a[20:23]\n\t"
      "v_exp_f32 v158, v158\n\t"
      "v_mfma_f32_16x16x32_bf16 v[124:127], a[180:183], a[108:111], v[188:191]\n\t"
      "v_exp_f32 v159, v159\n\t"
      "v_mfma_f32_16x16x32_bf16 v[124:127], a[184:187], a[112:115], v[124:127]\n\t"
      "v_exp_f32 v160, v160\n\t"
      "v_mfma_f32_16x16x32_bf16 v[124:127], a[188:191], a[116:119], v[124:127]\n\t"
      "global_load_lds_dwordx4 v220, s[58:59] offset:1024 sc1\n\t"
      "v_mfma_f32_16x16x32_bf16 v[128:131], a[200:203], a[116:119], v[188:191]\n\t"
      "v_exp_f32 v161, v161\n\t"
      "v_mfma_f32_16x16x32_bf16 v[128:131], a[196:199], a[112:115], v[128:131]\n\t"
      "v_cvt_pk_bf16_f32 v179, v154, v155\n\t"
      "v_cvt_pk_bf16_f32 v180, v156, v157\n\t"
      "v_mfma_f32_16x16x32_bf16 v[128:131], a[192:195], a[108:111], v[128:131]\n\t"
      "v_exp_f32 v162, v162\n\t"
      "v_mfma_f32_16x16x32_bf16 v[132:135], a[204:207], a[108:111], v[188:191]\n\t"
      "global_load_lds_dwordx4 v220, s[58:59] offset:2048 sc1\n\t"
      "v_mfma_f32_16x16x32_bf16 v[132:135], a[208:211], a[112:115], v[132:135]\n\t"
      "s_add_u32 s58, s58, 57344\n\t"
      "s_addc_u32 s59, s59, 0\n\t"
      "v_mfma_f32_16x16x32_bf16 v[132:135], a[212:215], a[116:119], v[132:135]\n\t"
      "v_exp_f32 v163, v163\n\t"
      "v_mfma_f32_16x16x32_bf16 v[136:139], a[224:227], a[116:119], v[188:191]\n\t"
      "v_exp_f32 v124, v124\n\t"
      "v_mfma_f32_16x16x32_bf16 v[136:139], a[220:223], a[112:115], v[136:139]\n\t"
      "global_load_dword v222, v221, s[58:59] sc1\n\t"
      "v_mfma_f32_16x16x32_bf16 v[136:139], a[216:219], a[108:111], v[136:139]\n\t"
      "v_exp_f32 v125, v125\n\t"
      "v_smfmac_f32_16x16x64_bf16 a[240:243], v[212:215], v[172:179], v216\n\t"
      "v_cvt_pk_bf16_f32 v181, v158, v159\n\t"
      "v_cvt_pk_bf16_f32 v182, v160, v161\n\t"
      "v_mfma_f32_16x16x32_bf16 v[4:7], v[100:103], v[176:179], v[4:7]\n\t"
      "v_xor_b32 v218, 0x10000, v218\n\t"
      "v_cvt_pk_bf16_f32 v183, v162, v163\n\t"
      "v_mfma_f32_16x16x32_bf16 v[4:7], v[76:79], v[172:175], v[4:7]\n\t"
      "v_exp_f32 v126, v126\n\t"
      "v_mfma_f32_16x16x32_bf16 v[8:11], v[80:83], v[172:175], v[8:11]\n\t"
      "v_exp_f32 v127, v127\n\t"
      "v_mfma_f32_16x16x32_bf16 v[8:11], v[104:107], v[176:179], v[8:11]\n\t"
      "v_exp_f32 v128, v128\n\t"
      "v_mfma_f32_16x16x32_bf16 v[12:15], v[108:111], v[176:179], v[12:15]\n\t"
      "v_exp_f32 v129, v129\n\t"
      "v_mfma_f32_16x16x32_bf16 v[12:15], v[84:87], v[172:175], v[12:15]\n\t"
      "v_exp_f32 v130, v130\n\t"
      "v_mfma_f32_16x16x32_bf16 v[16:19], v[88:91], v[172:175], v[16:19]\n\t"
      "v_exp_f32 v131, v131\n\t"
      "v_mfma_f32_16x16x32_bf16 v[16:19], v[112:115], v[176:179], v[16:19]\n\t"
      "v_cvt_pk_bf16_f32 v164, v124, v125\n\t"
      "v_cvt_pk_bf16_f32 v165, v126, v127\n\t"
      "v_mfma_f32_16x16x32_bf16 v[20:23], v[116:119], v[176:179], v[20:23]\n\t"
      "v_exp_f32 v132, v132\n\t"
      "v_mfma_f32_16x16x32_bf16 v[20:23], v[92:95], v[172:175], v[20:23]\n\t"
      "v_exp_f32 v133, v133\n\t"
      "v_mfma_f32_16x16x32_bf16 v[24:27], v[96:99], v[172:175], v[24:27]\n\t"
      "v_exp_f32 v134, v134\n\t"
      "v_mfma_f32_16x16x32_bf16 v[24:27], v[120:123], v[176:179], v[24:27]\n\t"
      "v_exp_f32 v135, v135\n\t"
      "v_mfma_f32_16x16x32_bf16 v[140:143], a[180:183], a[144:147], v[200:203]\n\t"
      "v_cvt_pk_bf16_f32 v166, v128, v129\n\t"
      "v_cvt_pk_bf16_f32 v167, v130, v131\n\t"
      "v_mfma_f32_16x16x32_bf16 v[140:143], a[184:187], a[148:151], v[140:143]\n\t"
      "v_exp_f32 v136, v136\n\t"
      "v_mfma_f32_16x16x32_bf16 v[140:143], a[188:191], a[152:155], v[140:143]\n\t"
      "v_exp_f32 v137, v137\n\t"
      "v_mfma_f32_16x16x32_bf16 v[144:147], a[200:203], a[152:155], v[200:203]\n\t"
      "v_exp_f32 v138, v138\n\t"
      "v_mfma_f32_16x16x32_bf16 v[144:147], a[196:199], a[148:151], v[144:147]\n\t"
      "v_exp_f32 v139, v139\n\t"
      "v_mfma_f32_16x16x32_bf16 v[144:147], a[192:195], a[144:147], v[144:147]\n\t"
      "v_cvt_pk_bf16_f32 v168, v132, v133\n\t"
      "v_cvt_pk_bf16_f32 v169, v134, v135\n\t"
      "v_mfma_f32_16x16x32_bf16 v[148:151], a[204:207], a[144:147], v[200:203]\n\t"
      "v_exp_f32 v140, v140\n\t"
      "v_mfma_f32_16x16x32_bf16 v[148:151], a[208:211], a[148:151], v[148:151]\n\t"
      "v_exp_f32 v141, v141\n\t"
      "v_mfma_f32_16x16x32_bf16 v[148:151], a[212:215], a[152:155], v[148:151]\n\t"
      "v_exp_f32 v142, v142\n\t"
      "v_mfma_f32_16x16x32_bf16 v[152:155], a[224:227], a[152:155], v[200:203]\n\t"
      "v_exp_f32 v143, v143\n\t"
      "v_mfma_f32_16x16x32_bf16 v[152:155], a[220:223], a[148:151], v[152:155]\n\t"
      "v_cvt_pk_bf16_f32 v170, v136, v137\n\t"
      "v_cvt_pk_bf16_f32 v171, v138, v139\n\t"
      "v_mfma_f32_16x16x32_bf16 v[152:155], a[216:219], a[144:147], v[152:155]\n\t"
      "v_exp_f32 v144, v144\n\t"
      "v_mfma_f32_16x16x32_bf16 a[252:255], v[212:215], v[180:183], a[252:255]\n\t"
      "v_exp_f32 v145, v145\n\t"
      "v_mfma_f32_16x16x32_bf16 a[72:75], v[76:79], v[180:183], a[72:75]\n\t"
      "v_exp_f32 v146, v146\n\t"
      "v_mfma_f32_16x16x32_bf16 a[76:79], v[80:83], v[180:183], a[76:79]\n\t"
      "v_exp_f32 v147, v147\n\t"
      "v_mfma_f32_16x16x32_bf16 a[80:83], v[84:87], v[180:183], a[80:83]\n\t"
      "v_cvt_pk_bf16_f32 v172, v140, v141\n\t"
      "v_cvt_pk_bf16_f32 v173, v142, v143\n\t"
      "v_mfma_f32_16x16x32_bf16 a[84:87], v[88:91], v[180:183], a[84:87]\n\t"
      "v_exp_f32 v148, v148\n\t"
      "v_mfma_f32_16x16x32_bf16 a[88:91], v[92:95], v[180:183], a[88:91]\n\t"
      "v_exp_f32 v149, v149\n\t"
      "v_mfma_f32_16x16x32_bf16 a[92:95], v[96:99], v[180:183], a[92:95]\n\t"
      "v_exp_f32 v150, v150\n\t"
      "v_mfma_f32_16x16x32_bf16 v[124:127], a[180:183], a[120:123], v[192:195]\n\t"
      "v_exp_f32 v151, v151\n\t"
      "v_mfma_f32_16x16x32_bf16 v[124:127], a[184:187], a[124:127], v[124:127]\n\t"
      "v_cvt_pk_bf16_f32 v174, v144, v145\n\t"
      "v_cvt_pk_bf16_f32 v175, v146, v147\n\t"
      "v_mfma_f32_16x16x32_bf16 v[124:127], a[188:191], a[128:131], v[124:127]\n\t"
      "v_exp_f32 v152, v152\n\t"
      "v_mfma_f32_16x16x32_bf16 v[128:131], a[200:203], a[128:131], v[192:195]\n\t"
      "v_exp_f32 v153, v153\n\t"
      "v_mfma_f32_16x16x32_bf16 v[128:131], a[196:199], a[124:127], v[128:131]\n\t"
      "v_exp_f32 v154, v154\n\t"
      "v_mfma_f32_16x16x32_bf16 v[128:131], a[192:195], a[120:123], v[128:131]\n\t"
      "v_exp_f32 v155, v155\n\t"
      "v_mfma_f32_16x16x32_bf16 v[132:135], a[204:207], a[120:123], v[192:195]\n\t"
      "v_cvt_pk_bf16_f32 v176, v148, v149\n\t"
      "v_cvt_pk_bf16_f32 v177, v150, v151\n\t"
      "v_mfma_f32_16x16x32_bf16 v[132:135], a[208:211], a[124:127], v[132:135]\n\t"
      "v_exp_f32 v124, v124\n\t"
      "v_mfma_f32_16x16x32_bf16 v[132:135], a[212:215], a[128:131], v[132:135]\n\t"
      "v_exp_f32 v125, v125\n\t"
      "v_mfma_f32_16x16x32_bf16 v[136:139], a[224:227], a[128:131], v[192:195]\n\t"
      "v_exp_f32 v126, v126\n\t"
      "v_mfma_f32_16x16x32_bf16 v[136:139], a[220:223], a[124:127], v[136:139]\n\t"
      "v_exp_f32 v127, v127\n\t"
      "v_mfma_f32_16x16x32_bf16 v[136:139], a[216:219], a[120:123], v[136:139]\n\t"
      "v_exp_f32 v128, v128\n\t"
      "v_smfmac_f32_16x16x64_bf16 a[232:235], v[212:215], v[164:171], v216\n\t"
      "v_exp_f32 v129, v129\n\t"
      "v_mfma_f32_16x16x32_bf16 a[24:27], v[100:103], v[168:171], a[24:27]\n\t"
      "v_exp_f32 v130, v130\n\t"
      "v_mfma_f32_16x16x32_bf16 a[24:27], v[76:79], v[164:167], a[24:27]\n\t"
      "v_exp_f32 v131, v131\n\t"
      "v_mfma_f32_16x16x32_bf16 a[28:31], v[80:83], v[164:167], a[28:31]\n\t"
      "v_exp_f32 v132, v132\n\t"
      "v_mfma_f32_16x16x32_bf16 a[28:31], v[104:107], v[168:171], a[28:31]\n\t"
      "v_exp_f32 v133, v133\n\t"
      "v_mfma_f32_16x16x32_bf16 a[32:35], v[108:111], v[168:171], a[32:35]\n\t"
      "v_exp_f32 v134, v134\n\t"
      "v_mfma_f32_16x16x32_bf16 a[32:35], v[84:87], v[164:167], a[32:35]\n\t"
      "v_exp_f32 v135, v135\n\t"
      "v_mfma_f32_16x16x32_bf16 a[36:39], v[88:91], v[164:167], a[36:39]\n\t"
      "v_exp_f32 v136, v136\n\t"
      "v_mfma_f32_16x16x32_bf16 a[36:39], v[112:115], v[168:171], a[36:39]\n\t"
      "v_exp_f32 v137, v137\n\t"
      "v_mfma_f32_16x16x32_bf16 a[40:43], v[116:119], v[168:171], a[40:43]\n\t"
      "v_exp_f32 v138, v138\n\t"
      "v_mfma_f32_16x16x32_bf16 a[40:43], v[92:95], v[164:167], a[40:43]\n\t"
      "v_exp_f32 v139, v139\n\t"
      "v_mfma_f32_16x16x32_bf16 a[44:47], v[96:99], v[164:167], a[44:47]\n\t"
      "v_cvt_pk_bf16_f32 v178, v152, v153\n\t"
      "v_cvt_pk_bf16_f32 v179, v154, v155\n\t"
      "v_mfma_f32_16x16x32_bf16 a[44:47], v[120:123], v[168:171], a[44:47]\n\t"
      "v_cvt_pk_bf16_f32 v164, v124, v125\n\t"
      "v_cvt_pk_bf16_f32 v165, v126, v127\n\t"
      "v_mfma_f32_16x16x32_bf16 v[140:143], a[180:183], a[156:159], v[204:207]\n\t"
      "v_cvt_pk_bf16_f32 v166, v128, v129\n\t"
      "v_cvt_pk_bf16_f32 v167, v130, v131\n\t"
      "v_mfma_f32_16x16x32_bf16 v[140:143], a[184:187], a[160:163], v[140:143]\n\t"
      "v_cvt_pk_bf16_f32 v168, v132, v133\n\t"
      "v_cvt_pk_bf16_f32 v169, v134, v135\n\t"
      "v_mfma_f32_16x16x32_bf16 v[140:143], a[188:191], a[164:167], v[140:143]\n\t"
      "s_waitcnt vmcnt(0)\n\t"
      "s_barrier\n\t"
      "v_mfma_f32_16x16x32_bf16 v[144:147], a[200:203], a[164:167], v[204:207]\n\t"
      "ds_read_b128 a[180:183], v217 offset:0\n\t"
      "ds_read_b128 a[184:187], v217 offset:1024\n\t"
      "v_mfma_f32_16x16x32_bf16 v[144:147], a[196:199], a[160:163], v[144:147]\n\t"
      "v_cvt_pk_bf16_f32 v170, v136, v137\n\t"
      "v_cvt_pk_bf16_f32 v171, v138, v139\n\t"
      "v_mfma_f32_16x16x32_bf16 v[144:147], a[192:195], a[156:159], v[144:147]\n\t"
      "ds_read_b128 a[188:191], v217 offset:2048\n\t"
      "ds_read_b128 a[200:203], v217 offset:5120\n\t"
      "v_mfma_f32_16x16x32_bf16 v[148:151], a[204:207], a[156:159], v[204:207]\n\t"
      "v_exp_f32 v140, v140\n\t"
      "v_mfma_f32_16x16x32_bf16 v[148:151], a[208:211], a[160:163], v[148:151]\n\t"
      "v_exp_f32 v141, v141\n\t"
      "v_mfma_f32_16x16x32_bf16 v[148:151], a[212:215], a[164:167], v[148:151]\n\t"
      "ds_read_b128 a[196:199], v217 offset:4096\n\t"
      "ds_read_b128 a[192:195], v217 offset:3072\n\t"
      "v_mfma_f32_16x16x32_bf16 v[152:155], a[224:227], a[164:167], v[204:207]\n\t"
      "v_exp_f32 v142, v142\n\t"
      "v_mfma_f32_16x16x32_bf16 v[152:155], a[220:223], a[160:163], v[152:155]\n\t"
      "v_exp_f32 v143, v143\n\t"
      "v_mfma_f32_16x16x32_bf16 v[152:155], a[216:219], a[156:159], v[152:155]\n\t"
      "ds_read_b128 a[204:207], v218 offset:0\n\t"
      "ds_read_b128 a[208:211], v218 offset:1024\n\t"
      "v_smfmac_f32_16x16x64_bf16 a[244:247], v[212:215], v[172:179], v216\n\t"
      "v_exp_f32 v144, v144\n\t"
      "v_mfma_f32_16x16x32_bf16 v[28:31], v[100:103], v[176:179], v[28:31]\n\t"
      "ds_read_b128 a[212:215], v218 offset:2048\n\t"
      "ds_read_b128 a[224:227], v218 offset:5120\n\t"
      "v_mfma_f32_16x16x32_bf16 v[28:31], v[76:79], v[172:175], v[28:31]\n\t"
      "v_exp_f32 v145, v145\n\t"
      "v_mfma_f32_16x16x32_bf16 v[32:35], v[80:83], v[172:175], v[32:35]\n\t"
      "ds_read_b128 a[220:223], v218 offset:4096\n\t"
      "ds_read_b128 a[216:219], v218 offset:3072\n\t"
      "v_mfma_f32_16x16x32_bf16 v[32:35], v[104:107], v[176:179], v[32:35]\n\t"
      "v_exp_f32 v146, v146\n\t"
      "v_mfma_f32_16x16x32_bf16 v[36:39], v[108:111], v[176:179], v[36:39]\n\t"
      "v_exp_f32 v147, v147\n\t"
      "v_mfma_f32_16x16x32_bf16 v[36:39], v[84:87], v[172:175], v[36:39]\n\t"
      "v_exp_f32 v148, v148\n\t"
      "v_mfma_f32_16x16x32_bf16 v[40:43], v[88:91], v[172:175], v[40:43]\n\t"
      "v_exp_f32 v149, v149\n\t"
      "v_mfma_f32_16x16x32_bf16 v[40:43], v[112:115], v[176:179], v[40:43]\n\t"
      "v_exp_f32 v150, v150\n\t"
      "v_mfma_f32_16x16x32_bf16 v[44:47], v[116:119], v[176:179], v[44:47]\n\t"
      "v_cmp_ne_u32 vcc, s54, v222\n\t"
      "s_mov_b32 m0, s42\n\t"
      "v_mfma_f32_16x16x32_bf16 v[44:47], v[92:95], v[172:175], v[44:47]\n\t"
      "s_xor_b32 s42, s42, 0x10000\n\t"
      "s_cbranch_vccnz LslowH%=\n\t"
      "LbackH%=:\n\t"
      "v_mfma_f32_16x16x32_bf16 v[48:51], v[96:99], v[172:175], v[48:51]\n\t"
      "v_exp_f32 v151, v151\n\t"
      "v_mfma_f32_16x16x32_bf16 v[48:51], v[120:123], v[176:179], v[48:51]\n\t"
      "v_cvt_pk_bf16_f32 v172, v140, v141\n\t"
      "s_waitcnt lgkmcnt(6)\n\t"
      "v_mfma_f32_16x16x32_bf16 v[124:127], a[180:183], a[96:99], v[184:187]\n\t"
      "v_cvt_pk_bf16_f32 v173, v142, v143\n\t"
      "v_cvt_pk_bf16_f32 v174, v144, v145\n\t"
      "v_mfma_f32_16x16x32_bf16 v[124:127], a[184:187], a[100:103], v[124:127]\n\t"
      "global_load_lds_dwordx4 v219, s[58:59] offset:-3072 sc1\n\t"
      "v_mfma_f32_16x16x32_bf16 v[124:127], a[188:191], a[104:107], v[124:127]\n\t"
      "v_exp_f32 v152, v152\n\t"
      "v_mfma_f32_16x16x32_bf16 v[128:131], a[200:203], a[104:107], v[184:187]\n\t"
      "v_exp_f32 v153, v153\n\t"
      "v_mfma_f32_16x16x32_bf16 v[128:131], a[196:199], a[100:103], v[128:131]\n\t"
      "v_cvt_pk_bf16_f32 v175, v146, v147\n\t"
      "v_cvt_pk_bf16_f32 v176, v148, v149\n\t"
      "v_mfma_f32_16x16x32_bf16 v[128:131], a[192:195], a[96:99], v[128:131]\n\t"
      "v_cvt_pk_bf16_f32 v177, v150, v151\n\t"
      "s_waitcnt lgkmcnt(0)\n\t"
      "v_mfma_f32_16x16x32_bf16 v[132:135], a[204:207], a[96:99], v[184:187]\n\t"
      "global_load_lds_dwordx4 v219, s[58:59] offset:-2048 sc1\n\t"
      "v_mfma_f32_16x16x32_bf16 v[132:135], a[208:211], a[100:103], v[132:135]\n\t"
      "v_exp_f32 v154, v154\n\t"
      "v_mfma_f32_16x16x32_bf16 v[132:135], a[212:215], a[104:107], v[132:135]\n\t"
      "v_exp_f32 v155, v155\n\t"
      "v_mfma_f32_16x16x32_bf16 v[136:139], a[224:227], a[104:107], v[184:187]\n\t"
      "v_exp_f32 v124, v124\n\t"
      "v_mfma_f32_16x16x32_bf16 v[136:139], a[220:223], a[100:103], v[136:139]\n\t"
      "global_load_lds_dwordx4 v219, s[58:59] offset:-1024 sc1\n\t"
      "v_mfma_f32_16x16x32_bf16 v[136:139], a[216:219], a[96:99], v[136:139]\n\t"
      "v_exp_f32 v125, v125\n\t"
      "v_smfmac_f32_16x16x64_bf16 a[236:239], v[212:215], v[164:171], v216\n\t"
      "v_exp_f32 v126, v126\n\t"
      "v_mfma_f32_16x16x32_bf16 a[48:51], v[100:103], v[168:171], a[48:51]\n\t"
      "v_exp_f32 v127, v127\n\t"
      "v_mfma_f32_16x16x32_bf16 a[48:51], v[76:79], v[164:167], a[48:51]\n\t"
      "global_load_lds_dwordx4 v219, s[58:59] offset:0 sc1\n\t"
      "v_mfma_f32_16x16x32_bf16 a[52:55], v[80:83], v[164:167], a[52:55]\n\t"
      "v_exp_f32 v128, v128\n\t"
      "v_mfma_f32_16x16x32_bf16 a[52:55], v[104:107], v[168:171], a[52:55]\n\t"
      "v_exp_f32 v129, v129\n\t"
      "v_mfma_f32_16x16x32_bf16 a[56:59], v[108:111], v[168:171], a[56:59]\n\t"
      "v_exp_f32 v130, v130\n\t"
      "v_mfma_f32_16x16x32_bf16 a[56:59], v[84:87], v[164:167], a[56:59]\n\t"
      "global_load_lds_dwordx4 v219, s[58:59] offset:1024 sc1\n\t"
      "v_mfma_f32_16x16x32_bf16 a[60:63], v[88:91], v[164:167], a[60:63]\n\t"
      "v_exp_f32 v131, v131\n\t"
      "v_mfma_f32_16x16x32_bf16 a[60:63], v[112:115], v[168:171], a[60:63]\n\t"
      "v_exp_f32 v132, v132\n\t"
      "v_mfma_f32_16x16x32_bf16 a[64:67], v[116:119], v[168:171], a[64:67]\n\t"
      "v_exp_f32 v133, v133\n\t"
      "v_mfma_f32_16x16x32_bf16 a[64:67], v[92:95], v[164:167], a[64:67]\n\t"
      "global_load_lds_dwordx4 v219, s[58:59] offset:2048 sc1\n\t"
      "v_mfma_f32_16x16x32_bf16 a[68:71], v[96:99], v[164:167], a[68:71]\n\t"
      "v_exp_f32 v134, v134\n\t"
      "v_mfma_f32_16x16x32_bf16 a[68:71], v[120:123], v[168:171], a[68:71]\n\t"
      "v_cvt_pk_bf16_f32 v178, v152, v153\n\t"
      "v_cvt_pk_bf16_f32 v179, v154, v155\n\t"
      "v_mfma_f32_16x16x32_bf16 v[140:143], a[180:183], a[132:135], v[196:199]\n\t"
      "v_cvt_pk_bf16_f32 v164, v124, v125\n\t"
      "v_cvt_pk_bf16_f32 v165, v126, v127\n\t"
      "v_mfma_f32_16x16x32_bf16 v[140:143], a[184:187], a[136:139], v[140:143]\n\t"
      "v_exp_f32 v135, v135\n\t"
      "v_mfma_f32_16x16x32_bf16 v[140:143], a[188:191], a[140:143], v[140:143]\n\t"
      "v_cvt_pk_bf16_f32 v166, v128, v129\n\t"
      "v_cvt_pk_bf16_f32 v167, v130, v131\n\t"
      "v_mfma_f32_16x16x32_bf16 v[144:147], a[200:203], a[140:143], v[196:199]\n\t"
      "v_exp_f32 v136, v136\n\t"
      "v_mfma_f32_16x16x32_bf16 v[144:147], a[196:199], a[136:139], v[144:147]\n\t"
      "v_exp_f32 v137, v137\n\t"
      "v_mfma_f32_16x16x32_bf16 v[144:147], a[192:195], a[132:135], v[144:147]\n\t"
      "v_exp_f32 v138, v138\n\t"
      "v_mfma_f32_16x16x32_bf16 v[148:151], a[204:207], a[132:135], v[196:199]\n\t"
      "v_exp_f32 v139, v139\n\t"
      "v_mfma_f32_16x16x32_bf16 v[148:151], a[208:211], a[136:139], v[148:151]\n\t"
      "v_cvt_pk_bf16_f32 v168, v132, v133\n\t"
      "v_cvt_pk_bf16_f32 v169, v134, v135\n\t"
      "v_mfma_f32_16x16x32_bf16 v[148:151], a[212:215], a[140:143], v[148:151]\n\t"
      "v_exp_f32 v140, v140\n\t"
      "v_mfma_f32_16x16x32_bf16 v[152:155], a[224:227], a[140:143], v[196:199]\n\t"
      "v_exp_f32 v141, v141\n\t"
      "v_mfma_f32_16x16x32_bf16 v[152:155], a[220:223], a[136:139], v[152:155]\n\t"
      "v_exp_f32 v142, v142\n\t"
      "v_mfma_f32_16x16x32_bf16 v[152:155], a[216:219], a[132:135], v[152:155]\n\t"
      "v_exp_f32 v143, v143\n\t"
      "v_smfmac_f32_16x16x64_bf16 a[248:251], v[212:215], v[172:179], v216\n\t"
      "v_exp_f32 v144, v144\n\t"
      "v_mfma_f32_16x16x32_bf16 v[52:55], v[100:103], v[176:179], v[52:55]\n\t"
      "v_exp_f32 v145, v145\n\t"
      "v_mfma_f32_16x16x32_bf16 v[52:55], v[76:79], v[172:175], v[52:55]\n\t"
      "v_exp_f32 v146, v146\n\t"
      "v_mfma_f32_16x16x32_bf16 v[56:59], v[80:83], v[172:175], v[56:59]\n\t"
      "v_exp_f32 v147, v147\n\t"
      "v_mfma_f32_16x16x32_bf16 v[56:59], v[104:107], v[176:179], v[56:59]\n\t"
      "ds_read_b128 v[100:103], v218 offset:6144\n\t"
      "ds_read_b128 v[76:79], v217 offset:6144\n\t"
      "v_mfma_f32_16x16x32_bf16 v[60:63], v[108:111], v[176:179], v[60:63]\n\t"
      "v_exp_f32 v148, v148\n\t"
      "v_mfma_f32_16x16x32_bf16 v[60:63], v[84:87], v[172:175], v[60:63]\n\t"
      "ds_read_b128 v[80:83], v217 offset:7168\n\t"
      "ds_read_b128 v[104:107], v218 offset:7168\n\t"
      "v_mfma_f32_16x16x32_bf16 v[64:67], v[88:91], v[172:175], v[64:67]\n\t"
      "v_exp_f32 v149, v149\n\t"
      "v_mfma_f32_16x16x32_bf16 v[64:67], v[112:115], v[176:179], v[64:67]\n\t"
      "ds_read_b128 v[108:111], v218 offset:8192\n\t"
      "ds_read_b128 v[84:87], v217 offset:8192\n\t"
      "v_mfma_f32_16x16x32_bf16 v[68:71], v[116:119], v[176:179], v[68:71]\n\t"
      "v_exp_f32 v150, v150\n\t"
      "v_mfma_f32_16x16x32_bf16 v[68:71], v[92:95], v[172:175], v[68:71]\n\t"
      "ds_read_b128 v[88:91], v217 offset:9216\n\t"
      "ds_read_b128 v[112:115], v218 offset:9216\n\t"
      "v_mfma_f32_16x16x32_bf16 v[72:75], v[96:99], v[172:175], v[72:75]\n\t"
      "s_sub_u32 s44, s44, 1\n\t"
      "v_cvt_pk_bf16_f32 v170, v136, v137\n\t"
      "v_mfma_f32_16x16x32_bf16 v[72:75], v[120:123], v[176:179], v[72:75]\n\t"
      "v_cvt_pk_bf16_f32 v171, v138, v139\n\t"
      "s_cbranch_scc0 Lmain_loopH%=\n\t"
      "v_mfma_f32_16x16x32_bf16 v[156:159], a[180:183], a[168:171], v[208:211]\n\t"
      "ds_read_b128 v[116:119], v218 offset:10240\n\t"
      "ds_read_b128 v[92:95], v217 offset:10240\n\t"
      "v_mfma_f32_16x16x32_bf16 v[156:159], a[184:187], a[172:175], v[156:159]\n\t"
      "v_cvt_pk_bf16_f32 v172, v140, v141\n\t"
      "v_cvt_pk_bf16_f32 v173, v142, v143\n\t"
      "v_mfma_f32_16x16x32_bf16 v[156:159], a[188:191], a[176:179], v[156:159]\n\t"
      "ds_read_b128 v[96:99], v217 offset:11264\n\t"
      "ds_read_b128 v[120:123], v218 offset:11264\n\t"
      "v_mfma_f32_16x16x32_bf16 v[160:163], a[200:203], a[176:179], v[208:211]\n\t"
      "v_exp_f32 v151, v151\n\t"
      "v_mfma_f32_16x16x32_bf16 v[160:163], a[196:199], a[172:175], v[160:163]\n\t"
      "s_add_u32 m0, m0, 6144\n\t"
      "v_cvt_pk_bf16_f32 v174, v144, v145\n\t"
      "v_mfma_f32_16x16x32_bf16 v[160:163], a[192:195], a[168:171], v[160:163]\n\t"
      "global_load_lds_dwordx4 v220, s[58:59] offset:-3072 sc1\n\t"
      "v_smfmac_f32_16x16x64_bf16 a[228:231], v[212:215], v[164:171], v216\n\t"
      "v_xor_b32 v217, 0x10000, v217\n\t"
      "s_waitcnt lgkmcnt(0)\n\t"
      "v_mfma_f32_16x16x32_bf16 a[0:3], v[100:103], v[168:171], a[0:3]\n\t"
      "v_exp_f32 v152, v152\n\t"
      "v_mfma_f32_16x16x32_bf16 a[0:3], v[76:79], v[164:167], a[0:3]\n\t"
      "v_exp_f32 v153, v153\n\t"
      "v_mfma_f32_16x16x32_bf16 a[4:7], v[80:83], v[164:167], a[4:7]\n\t"
      "global_load_lds_dwordx4 v220, s[58:59] offset:-2048 sc1\n\t"
      "v_mfma_f32_16x16x32_bf16 a[4:7], v[104:107], v[168:171], a[4:7]\n\t"
      "v_cvt_pk_bf16_f32 v175, v146, v147\n\t"
      "v_cvt_pk_bf16_f32 v176, v148, v149\n\t"
      "v_mfma_f32_16x16x32_bf16 a[8:11], v[108:111], v[168:171], a[8:11]\n\t"
      "v_exp_f32 v154, v154\n\t"
      "v_mfma_f32_16x16x32_bf16 a[8:11], v[84:87], v[164:167], a[8:11]\n\t"
      "v_exp_f32 v155, v155\n\t"
      "v_mfma_f32_16x16x32_bf16 a[12:15], v[88:91], v[164:167], a[12:15]\n\t"
      "global_load_lds_dwordx4 v220, s[58:59] offset:-1024 sc1\n\t"
      "v_mfma_f32_16x16x32_bf16 a[12:15], v[112:115], v[168:171], a[12:15]\n\t"
      "v_exp_f32 v156, v156\n\t"
      "v_mfma_f32_16x16x32_bf16 a[16:19], v[116:119], v[168:171], a[16:19]\n\t"
      "v_exp_f32 v157, v157\n\t"
      "v_mfma_f32_16x16x32_bf16 a[16:19], v[92:95], v[164:167], a[16:19]\n\t"
      "v_cvt_pk_bf16_f32 v177, v150, v151\n\t"
      "v_cvt_pk_bf16_f32 v178, v152, v153\n\t"
      "v_mfma_f32_16x16x32_bf16 a[20:23], v[96:99], v[164:167], a[20:23]\n\t"
      "global_load_lds_dwordx4 v220, s[58:59] offset:0 sc1\n\t"
      "v_mfma_f32_16x16x32_bf16 a[20:23], v[120:123], v[168:171], a[20:23]\n\t"
      "v_exp_f32 v158, v158\n\t"
      "v_mfma_f32_16x16x32_bf16 v[124:127], a[180:183], a[108:111], v[188:191]\n\t"
      "v_exp_f32 v159, v159\n\t"
      "v_mfma_f32_16x16x32_bf16 v[124:127], a[184:187], a[112:115], v[124:127]\n\t"
      "v_exp_f32 v160, v160\n\t"
      "v_mfma_f32_16x16x32_bf16 v[124:127], a[188:191], a[116:119], v[124:127]\n\t"
      "global_load_lds_dwordx4 v220, s[58:59] offset:1024 sc1\n\t"
      "v_mfma_f32_16x16x32_bf16 v[128:131], a[200:203], a[116:119], v[188:191]\n\t"
      "v_exp_f32 v161, v161\n\t"
      "v_mfma_f32_16x16x32_bf16 v[128:131], a[196:199], a[112:115], v[128:131]\n\t"
      "v_cvt_pk_bf16_f32 v179, v154, v155\n\t"
      "v_cvt_pk_bf16_f32 v180, v156, v157\n\t"
      "v_mfma_f32_16x16x32_bf16 v[128:131], a[192:195], a[108:111], v[128:131]\n\t"
      "v_exp_f32 v162, v162\n\t"
      "v_mfma_f32_16x16x32_bf16 v[132:135], a[204:207], a[108:111], v[188:191]\n\t"
      "global_load_lds_dwordx4 v220, s[58:59] offset:2048 sc1\n\t"
      "v_mfma_f32_16x16x32_bf16 v[132:135], a[208:211], a[112:115], v[132:135]\n\t"
      "v_mfma_f32_16x16x32_bf16 v[132:135], a[212:215], a[116:119], v[132:135]\n\t"
      "v_exp_f32 v163, v163\n\t"
      "v_mfma_f32_16x16x32_bf16 v[136:139], a[224:227], a[116:119], v[188:191]\n\t"
      "v_exp_f32 v124, v124\n\t"
      "v_mfma_f32_16x16x32_bf16 v[136:139], a[220:223], a[112:115], v[136:139]\n\t"
      "v_mfma_f32_16x16x32_bf16 v[136:139], a[216:219], a[108:111], v[136:139]\n\t"
      "v_exp_f32 v125, v125\n\t"
      "v_smfmac_f32_16x16x64_bf16 a[240:243], v[212:215], v[172:179], v216\n\t"
      "v_cvt_pk_bf16_f32 v181, v158, v159\n\t"
      "v_cvt_pk_bf16_f32 v182, v160, v161\n\t"
      "v_mfma_f32_16x16x32_bf16 v[4:7], v[100:103], v[176:179], v[4:7]\n\t"
      "v_xor_b32 v218, 0x10000, v218\n\t"
      "v_cvt_pk_bf16_f32 v183, v162, v163\n\t"
      "v_mfma_f32_16x16x32_bf16 v[4:7], v[76:79], v[172:175], v[4:7]\n\t"
      "v_exp_f32 v126, v126\n\t"
      "v_mfma_f32_16x16x32_bf16 v[8:11], v[80:83], v[172:175], v[8:11]\n\t"
      "v_exp_f32 v127, v127\n\t"
      "v_mfma_f32_16x16x32_bf16 v[8:11], v[104:107], v[176:179], v[8:11]\n\t"
      "v_exp_f32 v128, v128\n\t"
      "v_mfma_f32_16x16x32_bf16 v[12:15], v[108:111], v[176:179], v[12:15]\n\t"
      "v_exp_f32 v129, v129\n\t"
      "v_mfma_f32_16x16x32_bf16 v[12:15], v[84:87], v[172:175], v[12:15]\n\t"
      "v_exp_f32 v130, v130\n\t"
      "v_mfma_f32_16x16x32_bf16 v[16:19], v[88:91], v[172:175], v[16:19]\n\t"
      "v_exp_f32 v131, v131\n\t"
      "v_mfma_f32_16x16x32_bf16 v[16:19], v[112:115], v[176:179], v[16:19]\n\t"
      "v_cvt_pk_bf16_f32 v164, v124, v125\n\t"
      "v_cvt_pk_bf16_f32 v165, v126, v127\n\t"
      "v_mfma_f32_16x16x32_bf16 v[20:23], v[116:119], v[176:179], v[20:23]\n\t"
      "v_exp_f32 v132, v132\n\t"
      "v_mfma_f32_16x16x32_bf16 v[20:23], v[92:95], v[172:175], v[20:23]\n\t"
      "v_exp_f32 v133, v133\n\t"
      "v_mfma_f32_16x16x32_bf16 v[24:27], v[96:99], v[172:175], v[24:27]\n\t"
      "v_exp_f32 v134, v134\n\t"
      "v_mfma_f32_16x16x32_bf16 v[24:27], v[120:123], v[176:179], v[24:27]\n\t"
      "v_exp_f32 v135, v135\n\t"
      "v_mfma_f32_16x16x32_bf16 v[140:143], a[180:183], a[144:147], v[200:203]\n\t"
      "v_cvt_pk_bf16_f32 v166, v128, v129\n\t"
      "v_cvt_pk_bf16_f32 v167, v130, v131\n\t"
      "v_mfma_f32_16x16x32_bf16 v[140:143], a[184:187], a[148:151], v[140:143]\n\t"
      "v_exp_f32 v136, v136\n\t"
      "v_mfma_f32_16x16x32_bf16 v[140:143], a[188:191], a[152:155], v[140:143]\n\t"
      "v_exp_f32 v137, v137\n\t"
      "v_mfma_f32_16x16x32_bf16 v[144:147], a[200:203], a[152:155], v[200:203]\n\t"
      "v_exp_f32 v138, v138\n\t"
      "v_mfma_f32_16x16x32_bf16 v[144:147], a[196:199], a[148:151], v[144:147]\n\t"
      "v_exp_f32 v139, v139\n\t"
      "v_mfma_f32_16x16x32_bf16 v[144:147], a[192:195], a[144:147], v[144:147]\n\t"
      "v_cvt_pk_bf16_f32 v168, v132, v133\n\t"
      "v_cvt_pk_bf16_f32 v169, v134, v135\n\t"
      "v_mfma_f32_16x16x32_bf16 v[148:151], a[204:207], a[144:147], v[200:203]\n\t"
      "v_exp_f32 v140, v140\n\t"
      "v_mfma_f32_16x16x32_bf16 v[148:151], a[208:211], a[148:151], v[148:151]\n\t"
      "v_exp_f32 v141, v141\n\t"
      "v_mfma_f32_16x16x32_bf16 v[148:151], a[212:215], a[152:155], v[148:151]\n\t"
      "v_exp_f32 v142, v142\n\t"
      "v_mfma_f32_16x16x32_bf16 v[152:155], a[224:227], a[152:155], v[200:203]\n\t"
      "v_exp_f32 v143, v143\n\t"
      "v_mfma_f32_16x16x32_bf16 v[152:155], a[220:223], a[148:151], v[152:155]\n\t"
      "v_cvt_pk_bf16_f32 v170, v136, v137\n\t"
      "v_cvt_pk_bf16_f32 v171, v138, v139\n\t"
      "v_mfma_f32_16x16x32_bf16 v[152:155], a[216:219], a[144:147], v[152:155]\n\t"
      "v_exp_f32 v144, v144\n\t"
      "v_mfma_f32_16x16x32_bf16 a[252:255], v[212:215], v[180:183], a[252:255]\n\t"
      "v_exp_f32 v145, v145\n\t"
      "v_mfma_f32_16x16x32_bf16 a[72:75], v[76:79], v[180:183], a[72:75]\n\t"
      "v_exp_f32 v146, v146\n\t"
      "v_mfma_f32_16x16x32_bf16 a[76:79], v[80:83], v[180:183], a[76:79]\n\t"
      "v_exp_f32 v147, v147\n\t"
      "v_mfma_f32_16x16x32_bf16 a[80:83], v[84:87], v[180:183], a[80:83]\n\t"
      "v_cvt_pk_bf16_f32 v172, v140, v141\n\t"
      "v_cvt_pk_bf16_f32 v173, v142, v143\n\t"
      "v_mfma_f32_16x16x32_bf16 a[84:87], v[88:91], v[180:183], a[84:87]\n\t"
      "v_exp_f32 v148, v148\n\t"
      "v_mfma_f32_16x16x32_bf16 a[88:91], v[92:95], v[180:183], a[88:91]\n\t"
      "v_exp_f32 v149, v149\n\t"
      "v_mfma_f32_16x16x32_bf16 a[92:95], v[96:99], v[180:183], a[92:95]\n\t"
      "v_exp_f32 v150, v150\n\t"
      "v_mfma_f32_16x16x32_bf16 v[124:127], a[180:183], a[120:123], v[192:195]\n\t"
      "v_exp_f32 v151, v151\n\t"
      "v_mfma_f32_16x16x32_bf16 v[124:127], a[184:187], a[124:127], v[124:127]\n\t"
      "v_cvt_pk_bf16_f32 v174, v144, v145\n\t"
      "v_cvt_pk_bf16_f32 v175, v146, v147\n\t"
      "v_mfma_f32_16x16x32_bf16 v[124:127], a[188:191], a[128:131], v[124:127]\n\t"
      "v_exp_f32 v152, v152\n\t"
      "v_mfma_f32_16x16x32_bf16 v[128:131], a[200:203], a[128:131], v[192:195]\n\t"
      "v_exp_f32 v153, v153\n\t"
      "v_mfma_f32_16x16x32_bf16 v[128:131], a[196:199], a[124:127], v[128:131]\n\t"
      "v_exp_f32 v154, v154\n\t"
      "v_mfma_f32_16x16x32_bf16 v[128:131], a[192:195], a[120:123], v[128:131]\n\t"
      "v_exp_f32 v155, v155\n\t"
      "v_mfma_f32_16x16x32_bf16 v[132:135], a[204:207], a[120:123], v[192:195]\n\t"
      "v_cvt_pk_bf16_f32 v176, v148, v149\n\t"
      "v_cvt_pk_bf16_f32 v177, v150, v151\n\t"
      "v_mfma_f32_16x16x32_bf16 v[132:135], a[208:211], a[124:127], v[132:135]\n\t"
      "v_exp_f32 v124, v124\n\t"
      "v_mfma_f32_16x16x32_bf16 v[132:135], a[212:215], a[128:131], v[132:135]\n\t"
      "v_exp_f32 v125, v125\n\t"
      "v_mfma_f32_16x16x32_bf16 v[136:139], a[224:227], a[128:131], v[192:195]\n\t"
      "v_exp_f32 v126, v126\n\t"
      "v_mfma_f32_16x16x32_bf16 v[136:139], a[220:223], a[124:127], v[136:139]\n\t"
      "v_exp_f32 v127, v127\n\t"
      "v_mfma_f32_16x16x32_bf16 v[136:139], a[216:219], a[120:123], v[136:139]\n\t"
      "v_exp_f32 v128, v128\n\t"
      "v_smfmac_f32_16x16x64_bf16 a[232:235], v[212:215], v[164:171], v216\n\t"
      "v_exp_f32 v129, v129\n\t"
      "v_mfma_f32_16x16x32_bf16 a[24:27], v[100:103], v[168:171], a[24:27]\n\t"
      "v_exp_f32 v130, v130\n\t"
      "v_mfma_f32_16x16x32_bf16 a[24:27], v[76:79], v[164:167], a[24:27]\n\t"
      "v_exp_f32 v131, v131\n\t"
      "v_mfma_f32_16x16x32_bf16 a[28:31], v[80:83], v[164:167], a[28:31]\n\t"
      "v_exp_f32 v132, v132\n\t"
      "v_mfma_f32_16x16x32_bf16 a[28:31], v[104:107], v[168:171], a[28:31]\n\t"
      "v_exp_f32 v133, v133\n\t"
      "v_mfma_f32_16x16x32_bf16 a[32:35], v[108:111], v[168:171], a[32:35]\n\t"
      "v_exp_f32 v134, v134\n\t"
      "v_mfma_f32_16x16x32_bf16 a[32:35], v[84:87], v[164:167], a[32:35]\n\t"
      "v_exp_f32 v135, v135\n\t"
      "v_mfma_f32_16x16x32_bf16 a[36:39], v[88:91], v[164:167], a[36:39]\n\t"
      "v_exp_f32 v136, v136\n\t"
      "v_mfma_f32_16x16x32_bf16 a[36:39], v[112:115], v[168:171], a[36:39]\n\t"
      "v_exp_f32 v137, v137\n\t"
      "v_mfma_f32_16x16x32_bf16 a[40:43], v[116:119], v[168:171], a[40:43]\n\t"
      "v_exp_f32 v138, v138\n\t"
      "v_mfma_f32_16x16x32_bf16 a[40:43], v[92:95], v[164:167], a[40:43]\n\t"
      "v_exp_f32 v139, v139\n\t"
      "v_mfma_f32_16x16x32_bf16 a[44:47], v[96:99], v[164:167], a[44:47]\n\t"
      "v_cvt_pk_bf16_f32 v178, v152, v153\n\t"
      "v_cvt_pk_bf16_f32 v179, v154, v155\n\t"
      "v_mfma_f32_16x16x32_bf16 a[44:47], v[120:123], v[168:171], a[44:47]\n\t"
      "v_cvt_pk_bf16_f32 v164, v124, v125\n\t"
      "v_cvt_pk_bf16_f32 v165, v126, v127\n\t"
      "v_mfma_f32_16x16x32_bf16 v[140:143], a[180:183], a[156:159], v[204:207]\n\t"
      "v_cvt_pk_bf16_f32 v166, v128, v129\n\t"
      "v_cvt_pk_bf16_f32 v167, v130, v131\n\t"
      "v_mfma_f32_16x16x32_bf16 v[140:143], a[184:187], a[160:163], v[140:143]\n\t"
      "v_cvt_pk_bf16_f32 v168, v132, v133\n\t"
      "v_cvt_pk_bf16_f32 v169, v134, v135\n\t"
      "v_mfma_f32_16x16x32_bf16 v[140:143], a[188:191], a[164:167], v[140:143]\n\t"
      "s_waitcnt vmcnt(0)\n\t"
      "s_barrier\n\t"
      "v_mfma_f32_16x16x32_bf16 v[144:147], a[200:203], a[164:167], v[204:207]\n\t"
      "ds_read_b128 a[180:183], v217 offset:0\n\t"
      "ds_read_b128 a[184:187], v217 offset:1024\n\t"
      "v_mfma_f32_16x16x32_bf16 v[144:147], a[196:199], a[160:163], v[144:147]\n\t"
      "v_cvt_pk_bf16_f32 v170, v136, v137\n\t"
      "v_cvt_pk_bf16_f32 v171, v138, v139\n\t"
      "v_mfma_f32_16x16x32_bf16 v[144:147], a[192:195], a[156:159], v[144:147]\n\t"
      "ds_read_b128 a[188:191], v217 offset:2048\n\t"
      "ds_read_b128 a[200:203], v217 offset:5120\n\t"
      "v_mfma_f32_16x16x32_bf16 v[148:151], a[204:207], a[156:159], v[204:207]\n\t"
      "v_exp_f32 v140, v140\n\t"
      "v_mfma_f32_16x16x32_bf16 v[148:151], a[208:211], a[160:163], v[148:151]\n\t"
      "v_exp_f32 v141, v141\n\t"
      "v_mfma_f32_16x16x32_bf16 v[148:151], a[212:215], a[164:167], v[148:151]\n\t"
      "ds_read_b128 a[196:199], v217 offset:4096\n\t"
      "ds_read_b128 a[192:195], v217 offset:3072\n\t"
      "v_mfma_f32_16x16x32_bf16 v[152:155], a[224:227], a[164:167], v[204:207]\n\t"
      "v_exp_f32 v142, v142\n\t"
      "v_mfma_f32_16x16x32_bf16 v[152:155], a[220:223], a[160:163], v[152:155]\n\t"
      "v_exp_f32 v143, v143\n\t"
      "v_mfma_f32_16x16x32_bf16 v[152:155], a[216:219], a[156:159], v[152:155]\n\t"
      "ds_read_b128 a[204:207], v218 offset:0\n\t"
      "ds_read_b128 a[208:211], v218 offset:1024\n\t"
      "v_smfmac_f32_16x16x64_bf16 a[244:247], v[212:215], v[172:179], v216\n\t"
      "v_exp_f32 v144, v144\n\t"
      "v_mfma_f32_16x16x32_bf16 v[28:31], v[100:103], v[176:179], v[28:31]\n\t"
      "ds_read_b128 a[212:215], v218 offset:2048\n\t"
      "ds_read_b128 a[224:227], v218 offset:5120\n\t"
      "v_mfma_f32_16x16x32_bf16 v[28:31], v[76:79], v[172:175], v[28:31]\n\t"
      "v_exp_f32 v145, v145\n\t"
      "v_mfma_f32_16x16x32_bf16 v[32:35], v[80:83], v[172:175], v[32:35]\n\t"
      "ds_read_b128 a[220:223], v218 offset:4096\n\t"
      "ds_read_b128 a[216:219], v218 offset:3072\n\t"
      "v_mfma_f32_16x16x32_bf16 v[32:35], v[104:107], v[176:179], v[32:35]\n\t"
      "v_exp_f32 v146, v146\n\t"
      "v_mfma_f32_16x16x32_bf16 v[36:39], v[108:111], v[176:179], v[36:39]\n\t"
      "v_exp_f32 v147, v147\n\t"
      "v_mfma_f32_16x16x32_bf16 v[36:39], v[84:87], v[172:175], v[36:39]\n\t"
      "v_exp_f32 v148, v148\n\t"
      "v_mfma_f32_16x16x32_bf16 v[40:43], v[88:91], v[172:175], v[40:43]\n\t"
      "v_exp_f32 v149, v149\n\t"
      "v_mfma_f32_16x16x32_bf16 v[40:43], v[112:115], v[176:179], v[40:43]\n\t"
      "v_exp_f32 v150, v150\n\t"
      "v_mfma_f32_16x16x32_bf16 v[44:47], v[116:119], v[176:179], v[44:47]\n\t"
      "v_mfma_f32_16x16x32_bf16 v[44:47], v[92:95], v[172:175], v[44:47]\n\t"
      "v_mfma_f32_16x16x32_bf16 v[48:51], v[96:99], v[172:175], v[48:51]\n\t"
      "v_exp_f32 v151, v151\n\t"
      "v_mfma_f32_16x16x32_bf16 v[48:51], v[120:123], v[176:179], v[48:51]\n\t"
      "v_cvt_pk_bf16_f32 v172, v140, v141\n\t"
      "s_waitcnt lgkmcnt(6)\n\t"
      "v_mfma_f32_16x16x32_bf16 v[124:127], a[180:183], a[96:99], v[184:187]\n\t"
      "v_cvt_pk_bf16_f32 v173, v142, v143\n\t"
      "v_cvt_pk_bf16_f32 v174, v144, v145\n\t"
      "v_mfma_f32_16x16x32_bf16 v[124:127], a[184:187], a[100:103], v[124:127]\n\t"
      "v_mfma_f32_16x16x32_bf16 v[124:127], a[188:191], a[104:107], v[124:127]\n\t"
      "v_exp_f32 v152, v152\n\t"
      "v_mfma_f32_16x16x32_bf16 v[128:131], a[200:203], a[104:107], v[184:187]\n\t"
      "v_exp_f32 v153, v153\n\t"
      "v_mfma_f32_16x16x32_bf16 v[128:131], a[196:199], a[100:103], v[128:131]\n\t"
      "v_cvt_pk_bf16_f32 v175, v146, v147\n\t"
      "v_cvt_pk_bf16_f32 v176, v148, v149\n\t"
      "v_mfma_f32_16x16x32_bf16 v[128:131], a[192:195], a[96:99], v[128:131]\n\t"
      "v_cvt_pk_bf16_f32 v177, v150, v151\n\t"
      "s_waitcnt lgkmcnt(0)\n\t"
      "v_mfma_f32_16x16x32_bf16 v[132:135], a[204:207], a[96:99], v[184:187]\n\t"
      "v_mfma_f32_16x16x32_bf16 v[132:135], a[208:211], a[100:103], v[132:135]\n\t"
      "v_exp_f32 v154, v154\n\t"
      "v_mfma_f32_16x16x32_bf16 v[132:135], a[212:215], a[104:107], v[132:135]\n\t"
      "v_exp_f32 v155, v155\n\t"
      "v_mfma_f32_16x16x32_bf16 v[136:139], a[224:227], a[104:107], v[184:187]\n\t"
      "v_exp_f32 v124, v124\n\t"
      "v_mfma_f32_16x16x32_bf16 v[136:139], a[220:223], a[100:103], v[136:139]\n\t"
      "v_mfma_f32_16x16x32_bf16 v[136:139], a[216:219], a[96:99], v[136:139]\n\t"
      "v_exp_f32 v125, v125\n\t"
      "v_smfmac_f32_16x16x64_bf16 a[236:239], v[212:215], v[164:171], v216\n\t"
      "v_exp_f32 v126, v126\n\t"
      "v_mfma_f32_16x16x32_bf16 a[48:51], v[100:103], v[168:171], a[48:51]\n\t"
      "v_exp_f32 v127, v127\n\t"
      "v_mfma_f32_16x16x32_bf16 a[48:51], v[76:79], v[164:167], a[48:51]\n\t"
      "v_mfma_f32_16x16x32_bf16 a[52:55], v[80:83], v[164:167], a[52:55]\n\t"
      "v_exp_f32 v128, v128\n\t"
      "v_mfma_f32_16x16x32_bf16 a[52:55], v[104:107], v[168:171], a[52:55]\n\t"
      "v_exp_f32 v129, v129\n\t"
      "v_mfma_f32_16x16x32_bf16 a[56:59], v[108:111], v[168:171], a[56:59]\n\t"
      "v_exp_f32 v130, v130\n\t"
      "v_mfma_f32_16x16x32_bf16 a[56:59], v[84:87], v[164:167], a[56:59]\n\t"
      "v_mfma_f32_16x16x32_bf16 a[60:63], v[88:91], v[164:167], a[60:63]\n\t"
      "v_exp_f32 v131, v131\n\t"
      "v_mfma_f32_16x16x32_bf16 a[60:63], v[112:115], v[168:171], a[60:63]\n\t"
      "v_exp_f32 v132, v132\n\t"
      "v_mfma_f32_16x16x32_bf16 a[64:67], v[116:119], v[168:171], a[64:67]\n\t"
      "v_exp_f32 v133, v133\n\t"
      "v_mfma_f32_16x16x32_bf16 a[64:67], v[92:95], v[164:167], a[64:67]\n\t"
      "v_mfma_f32_16x16x32_bf16 a[68:71], v[96:99], v[164:167], a[68:71]\n\t"
      "v_exp_f32 v134, v134\n\t"
      "v_mfma_f32_16x16x32_bf16 a[68:71], v[120:123], v[168:171], a[68:71]\n\t"
      "v_cvt_pk_bf16_f32 v178, v152, v153\n\t"
      "v_cvt_pk_bf16_f32 v179, v154, v155\n\t"
      "v_mfma_f32_16x16x32_bf16 v[140:143], a[180:183], a[132:135], v[196:199]\n\t"
      "v_cvt_pk_bf16_f32 v164, v124, v125\n\t"
      "v_cvt_pk_bf16_f32 v165, v126, v127\n\t"
      "v_mfma_f32_16x16x32_bf16 v[140:143], a[184:187], a[136:139], v[140:143]\n\t"
      "v_exp_f32 v135, v135\n\t"
      "v_mfma_f32_16x16x32_bf16 v[140:143], a[188:191], a[140:143], v[140:143]\n\t"
      "v_cvt_pk_bf16_f32 v166, v128, v129\n\t"
      "v_cvt_pk_bf16_f32 v167, v130, v131\n\t"
      "v_mfma_f32_16x16x32_bf16 v[144:147], a[200:203], a[140:143], v[196:199]\n\t"
      "v_exp_f32 v136, v136\n\t"
      "v_mfma_f32_16x16x32_bf16 v[144:147], a[196:199], a[136:139], v[144:147]\n\t"
      "v_exp_f32 v137, v137\n\t"
      "v_mfma_f32_16x16x32_bf16 v[144:147], a[192:195], a[132:135], v[144:147]\n\t"
      "v_exp_f32 v138, v138\n\t"
      "v_mfma_f32_16x16x32_bf16 v[148:151], a[204:207], a[132:135], v[196:199]\n\t"
      "v_exp_f32 v139, v139\n\t"
      "v_mfma_f32_16x16x32_bf16 v[148:151], a[208:211], a[136:139], v[148:151]\n\t"
      "v_cvt_pk_bf16_f32 v168, v132, v133\n\t"
      "v_cvt_pk_bf16_f32 v169, v134, v135\n\t"
      "v_mfma_f32_16x16x32_bf16 v[148:151], a[212:215], a[140:143], v[148:151]\n\t"
      "v_exp_f32 v140, v140\n\t"
      "v_mfma_f32_16x16x32_bf16 v[152:155], a[224:227], a[140:143], v[196:199]\n\t"
      "v_exp_f32 v141, v141\n\t"
      "v_mfma_f32_16x16x32_bf16 v[152:155], a[220:223], a[136:139], v[152:155]\n\t"
      "v_exp_f32 v142, v142\n\t"
      "v_mfma_f32_16x16x32_bf16 v[152:155], a[216:219], a[132:135], v[152:155]\n\t"
      "v_exp_f32 v143, v143\n\t"
      "v_smfmac_f32_16x16x64_bf16 a[248:251], v[212:215], v[172:179], v216\n\t"
      "v_exp_f32 v144, v144\n\t"
      "v_mfma_f32_16x16x32_bf16 v[52:55], v[100:103], v[176:179], v[52:55]\n\t"
      "v_exp_f32 v145, v145\n\t"
      "v_mfma_f32_16x16x32_bf16 v[52:55], v[76:79], v[172:175], v[52:55]\n\t"
      "v_exp_f32 v146, v146\n\t"
      "v_mfma_f32_16x16x32_bf16 v[56:59], v[80:83], v[172:175], v[56:59]\n\t"
      "v_exp_f32 v147, v147\n\t"
      "v_mfma_f32_16x16x32_bf16 v[56:59], v[104:107], v[176:179], v[56:59]\n\t"
      "ds_read_b128 v[100:103], v218 offset:6144\n\t"
      "ds_read_b128 v[76:79], v217 offset:6144\n\t"
      "v_mfma_f32_16x16x32_bf16 v[60:63], v[108:111], v[176:179], v[60:63]\n\t"
      "v_exp_f32 v148, v148\n\t"
      "v_mfma_f32_16x16x32_bf16 v[60:63], v[84:87], v[172:175], v[60:63]\n\t"
      "ds_read_b128 v[80:83], v217 offset:7168\n\t"
      "ds_read_b128 v[104:107], v218 offset:7168\n\t"
      "v_mfma_f32_16x16x32_bf16 v[64:67], v[88:91], v[172:175], v[64:67]\n\t"
      "v_exp_f32 v149, v149\n\t"
      "v_mfma_f32_16x16x32_bf16 v[64:67], v[112:115], v[176:179], v[64:67]\n\t"
      "ds_read_b128 v[108:111], v218 offset:8192\n\t"
      "ds_read_b128 v[84:87], v217 offset:8192\n\t"
      "v_mfma_f32_16x16x32_bf16 v[68:71], v[116:119], v[176:179], v[68:71]\n\t"
      "v_exp_f32 v150, v150\n\t"
      "v_mfma_f32_16x16x32_bf16 v[68:71], v[92:95], v[172:175], v[68:71]\n\t"
      "ds_read_b128 v[88:91], v217 offset:9216\n\t"
      "ds_read_b128 v[112:115], v218 offset:9216\n\t"
      "v_mfma_f32_16x16x32_bf16 v[72:75], v[96:99], v[172:175], v[72:75]\n\t"
      "v_cvt_pk_bf16_f32 v170, v136, v137\n\t"
      "v_mfma_f32_16x16x32_bf16 v[72:75], v[120:123], v[176:179], v[72:75]\n\t"
      "v_cvt_pk_bf16_f32 v171, v138, v139\n\t"
      "v_mfma_f32_16x16x32_bf16 v[156:159], a[180:183], a[168:171], v[208:211]\n\t"
      "ds_read_b128 v[116:119], v218 offset:10240\n\t"
      "ds_read_b128 v[92:95], v217 offset:10240\n\t"
      "v_mov_b32 v223, 524544\n\t"
      "global_load_dword v223, v223, s[40:41] sc1\n\t"
      "s_mov_b32 s36, 0x55555555\n\t"
      "s_mov_b32 s37, 0x55555555\n\t"
      "v_mfma_f32_16x16x32_bf16 v[156:159], a[184:187], a[172:175], v[156:159]\n\t"
      "v_cvt_pk_bf16_f32 v172, v140, v141\n\t"
      "v_cvt_pk_bf16_f32 v173, v142, v143\n\t"
      "v_mfma_f32_16x16x32_bf16 v[156:159], a[188:191], a[176:179], v[156:159]\n\t"
      "ds_read_b128 v[96:99], v217 offset:11264\n\t"
      "ds_read_b128 v[120:123], v218 offset:11264\n\t"
      "v_mfma_f32_16x16x32_bf16 v[160:163], a[200:203], a[176:179], v[208:211]\n\t"
      "v_exp_f32 v151, v151\n\t"
      "v_mfma_f32_16x16x32_bf16 v[160:163], a[196:199], a[172:175], v[160:163]\n\t"
      "v_cvt_pk_bf16_f32 v174, v144, v145\n\t"
      "v_mfma_f32_16x16x32_bf16 v[160:163], a[192:195], a[168:171], v[160:163]\n\t"
      "v_smfmac_f32_16x16x64_bf16 a[228:231], v[212:215], v[164:171], v216\n\t"
      "s_waitcnt lgkmcnt(0)\n\t"
      "v_mfma_f32_16x16x32_bf16 a[0:3], v[100:103], v[168:171], a[0:3]\n\t"
      "v_exp_f32 v152, v152\n\t"
      "v_mfma_f32_16x16x32_bf16 a[0:3], v[76:79], v[164:167], a[0:3]\n\t"
      "v_exp_f32 v153, v153\n\t"
      "v_mfma_f32_16x16x32_bf16 a[4:7], v[80:83], v[164:167], a[4:7]\n\t"
      "v_mfma_f32_16x16x32_bf16 a[4:7], v[104:107], v[168:171], a[4:7]\n\t"
      "v_cvt_pk_bf16_f32 v175, v146, v147\n\t"
      "v_cvt_pk_bf16_f32 v176, v148, v149\n\t"
      "v_accvgpr_read_b32 v249, a228\n\t"
      "v_accvgpr_read_b32 v250, a229\n\t"
      "v_add_f32 v249, v249, v250\n\t"
      "ds_write_b32 v244, v249 offset:0\n\t"
      "ds_write_b128 v240, a[0:3] offset:0\n\t"
      "v_mfma_f32_16x16x32_bf16 a[8:11], v[108:111], v[168:171], a[8:11]\n\t"
      "v_exp_f32 v154, v154\n\t"
      "v_mfma_f32_16x16x32_bf16 a[8:11], v[84:87], v[164:167], a[8:11]\n\t"
      "v_exp_f32 v155, v155\n\t"
      "ds_write_b128 v240, a[4:7] offset:1024\n\t"
      "v_mfma_f32_16x16x32_bf16 a[12:15], v[88:91], v[164:167], a[12:15]\n\t"
      "v_mfma_f32_16x16x32_bf16 a[12:15], v[112:115], v[168:171], a[12:15]\n\t"
      "v_exp_f32 v156, v156\n\t"
      "v_mfma_f32_16x16x32_bf16 a[16:19], v[116:119], v[168:171], a[16:19]\n\t"
      "v_exp_f32 v157, v157\n\t"
      "v_mfma_f32_16x16x32_bf16 a[16:19], v[92:95], v[164:167], a[16:19]\n\t"
      "v_cvt_pk_bf16_f32 v177, v150, v151\n\t"
      "v_cvt_pk_bf16_f32 v178, v152, v153\n\t"
      "ds_write_b128 v240, a[8:11] offset:2048\n\t"
      "v_mfma_f32_16x16x32_bf16 a[20:23], v[96:99], v[164:167], a[20:23]\n\t"
      "v_mfma_f32_16x16x32_bf16 a[20:23], v[120:123], v[168:171], a[20:23]\n\t"
      "v_exp_f32 v158, v158\n\t"
      "ds_write_b128 v240, a[12:15] offset:3072\n\t"
      "v_mfma_f32_16x16x32_bf16 v[124:127], a[180:183], a[108:111], v[188:191]\n\t"
      "v_exp_f32 v159, v159\n\t"
      "v_mfma_f32_16x16x32_bf16 v[124:127], a[184:187], a[112:115], v[124:127]\n\t"
      "v_exp_f32 v160, v160\n\t"
      "ds_write_b128 v240, a[16:19] offset:4096\n\t"
      "v_mfma_f32_16x16x32_bf16 v[124:127], a[188:191], a[116:119], v[124:127]\n\t"
      "v_mfma_f32_16x16x32_bf16 v[128:131], a[200:203], a[116:119], v[188:191]\n\t"
      "v_exp_f32 v161, v161\n\t"
      "ds_write_b128 v240, a[20:23] offset:5120\n\t"
      "v_mfma_f32_16x16x32_bf16 v[128:131], a[196:199], a[112:115], v[128:131]\n\t"
      "v_cvt_pk_bf16_f32 v179, v154, v155\n\t"
      "v_cvt_pk_bf16_f32 v180, v156, v157\n\t"
      "v_mfma_f32_16x16x32_bf16 v[128:131], a[192:195], a[108:111], v[128:131]\n\t"
      "v_exp_f32 v162, v162\n\t"
      "v_mfma_f32_16x16x32_bf16 v[132:135], a[204:207], a[108:111], v[188:191]\n\t"
      "v_mfma_f32_16x16x32_bf16 v[132:135], a[208:211], a[112:115], v[132:135]\n\t"
      "v_mfma_f32_16x16x32_bf16 v[132:135], a[212:215], a[116:119], v[132:135]\n\t"
      "v_exp_f32 v163, v163\n\t"
      "s_waitcnt lgkmcnt(0)\n\t"
      "s_barrier\n\t"
      "ds_read_b32 v221, v245 offset:0\n\t"
      "ds_read_b128 v[184:187], v241 offset:0\n\t"
      "ds_read_b128 v[196:199], v241 offset:1024\n\t"
      "v_mfma_f32_16x16x32_bf16 v[136:139], a[224:227], a[116:119], v[188:191]\n\t"
      "v_exp_f32 v124, v124\n\t"
      "ds_read_b128 v[224:227], v241 offset:2048\n\t"
      "ds_read_b128 v[228:231], v241 offset:3072\n\t"
      "ds_read_b128 v[232:235], v241 offset:4096\n\t"
      "ds_read_b128 v[236:239], v241 offset:5120\n\t"
      "v_mfma_f32_16x16x32_bf16 v[136:139], a[220:223], a[112:115], v[136:139]\n\t"
      "v_mfma_f32_16x16x32_bf16 v[136:139], a[216:219], a[108:111], v[136:139]\n\t"
      "v_exp_f32 v125, v125\n\t"
      "v_smfmac_f32_16x16x64_bf16 a[240:243], v[212:215], v[172:179], v216\n\t"
      "v_cvt_pk_bf16_f32 v181, v158, v159\n\t"
      "v_cvt_pk_bf16_f32 v182, v160, v161\n\t"
      "v_mfma_f32_16x16x32_bf16 v[4:7], v[100:103], v[176:179], v[4:7]\n\t"
      "v_cvt_pk_bf16_f32 v183, v162, v163\n\t"
      "v_mfma_f32_16x16x32_bf16 v[4:7], v[76:79], v[172:175], v[4:7]\n\t"
      "v_exp_f32 v126, v126\n\t"
      "v_mfma_f32_16x16x32_bf16 v[8:11], v[80:83], v[172:175], v[8:11]\n\t"
      "v_exp_f32 v127, v127\n\t"
      "v_accvgpr_read_b32 v219, a240\n\t"
      "v_accvgpr_read_b32 v220, a241\n\t"
      "v_add_f32 v219, v219, v220\n\t"
      "ds_bpermute_b32 v218, v247, v219\n\t"
      "s_waitcnt lgkmcnt(5)\n\t"
      "v_mfma_f32_16x16x32_bf16 v[8:11], v[104:107], v[176:179], v[8:11]\n\t"
      "v_exp_f32 v128, v128\n\t"
      "v_pk_add_f32 v[4:5], v[4:5], v[184:185]\n\t"
      "v_pk_add_f32 v[6:7], v[6:7], v[186:187]\n\t"
      "v_mfma_f32_16x16x32_bf16 v[12:15], v[108:111], v[176:179], v[12:15]\n\t"
      "v_exp_f32 v129, v129\n\t"
      "v_mfma_f32_16x16x32_bf16 v[12:15], v[84:87], v[172:175], v[12:15]\n\t"
      "v_exp_f32 v130, v130\n\t"
      "v_mfma_f32_16x16x32_bf16 v[16:19], v[88:91], v[172:175], v[16:19]\n\t"
      "v_exp_f32 v131, v131\n\t"
      "v_pk_add_f32 v[8:9], v[8:9], v[196:197]\n\t"
      "v_pk_add_f32 v[10:11], v[10:11], v[198:199]\n\t"
      "v_mfma_f32_16x16x32_bf16 v[16:19], v[112:115], v[176:179], v[16:19]\n\t"
      "v_cvt_pk_bf16_f32 v164, v124, v125\n\t"
      "v_cvt_pk_bf16_f32 v165, v126, v127\n\t"
      "s_waitcnt lgkmcnt(0)\n\t"
      "v_add_f32 v221, v221, v218\n\t"
      "v_subrev_u32 v217, 0.5, v221\n\t"
      "v_rcp_f32 v221, v221\n\t"
      "v_mfma_f32_16x16x32_bf16 v[20:23], v[116:119], v[176:179], v[20:23]\n\t"
      "v_exp_f32 v132, v132\n\t"
      "v_pk_mul_f32 v[4:5], v[4:5], v[220:221] op_sel:[0,1] op_sel_hi:[1,1]\n\t"
      "v_pk_mul_f32 v[6:7], v[6:7], v[220:221] op_sel:[0,1] op_sel_hi:[1,1]\n\t"
      "v_pk_mul_f32 v[8:9], v[8:9], v[220:221] op_sel:[0,1] op_sel_hi:[1,1]\n\t"
      "v_mfma_f32_16x16x32_bf16 v[20:23], v[92:95], v[172:175], v[20:23]\n\t"
      "v_exp_f32 v133, v133\n\t"
      "v_pk_mul_f32 v[10:11], v[10:11], v[220:221] op_sel:[0,1] op_sel_hi:[1,1]\n\t"
      "v_cvt_pk_bf16_f32 v4, v4, v5\n\t"
      "v_cvt_pk_bf16_f32 v5, v6, v7\n\t"
      "v_cvt_pk_bf16_f32 v6, v8, v9\n\t"
      "v_mfma_f32_16x16x32_bf16 v[24:27], v[96:99], v[172:175], v[24:27]\n\t"
      "v_exp_f32 v134, v134\n\t"
      "v_cvt_pk_bf16_f32 v7, v10, v11\n\t"
      "s_waitcnt lgkmcnt(3)\n\t"
      "v_pk_add_f32 v[12:13], v[12:13], v[224:225]\n\t"
      "v_pk_add_f32 v[14:15], v[14:15], v[226:227]\n\t"
      "v_mfma_f32_16x16x32_bf16 v[24:27], v[120:123], v[176:179], v[24:27]\n\t"
      "v_exp_f32 v135, v135\n\t"
      "v_pk_add_f32 v[16:17], v[16:17], v[228:229]\n\t"
      "v_pk_add_f32 v[18:19], v[18:19], v[230:231]\n\t"
      "v_pk_mul_f32 v[12:13], v[12:13], v[220:221] op_sel:[0,1] op_sel_hi:[1,1]\n\t"
      "v_mfma_f32_16x16x32_bf16 v[140:143], a[180:183], a[144:147], v[200:203]\n\t"
      "v_cvt_pk_bf16_f32 v166, v128, v129\n\t"
      "v_cvt_pk_bf16_f32 v167, v130, v131\n\t"
      "v_pk_mul_f32 v[14:15], v[14:15], v[220:221] op_sel:[0,1] op_sel_hi:[1,1]\n\t"
      "v_pk_mul_f32 v[16:17], v[16:17], v[220:221] op_sel:[0,1] op_sel_hi:[1,1]\n\t"
      "v_pk_mul_f32 v[18:19], v[18:19], v[220:221] op_sel:[0,1] op_sel_hi:[1,1]\n\t"
      "v_mfma_f32_16x16x32_bf16 v[140:143], a[184:187], a[148:151], v[140:143]\n\t"
      "v_exp_f32 v136, v136\n\t"
      "v_cvt_pk_bf16_f32 v12, v12, v13\n\t"
      "v_cvt_pk_bf16_f32 v13, v14, v15\n\t"
      "v_cvt_pk_bf16_f32 v14, v16, v17\n\t"
      "v_cvt_pk_bf16_f32 v15, v18, v19\n\t"
      "s_mov_b64 vcc, s[36:37]\n\t"
      "v_mfma_f32_16x16x32_bf16 v[140:143], a[188:191], a[152:155], v[140:143]\n\t"
      "v_exp_f32 v137, v137\n\t"
      "v_cndmask_b32_dpp v8, v12, v4, vcc quad_perm:[1,0,3,2] row_mask:0xf bank_mask:0xf\n\t"
      "v_cndmask_b32_dpp v9, v13, v5, vcc quad_perm:[1,0,3,2] row_mask:0xf bank_mask:0xf\n\t"
      "v_cndmask_b32_dpp v10, v14, v6, vcc quad_perm:[1,0,3,2] row_mask:0xf bank_mask:0xf\n\t"
      "v_cndmask_b32_dpp v11, v15, v7, vcc quad_perm:[1,0,3,2] row_mask:0xf bank_mask:0xf\n\t"
      "global_store_dwordx4 %1, v[8:11], s[22:23] offset:0 sc1\n\t"
      "v_mfma_f32_16x16x32_bf16 v[144:147], a[200:203], a[152:155], v[200:203]\n\t"
      "v_exp_f32 v138, v138\n\t"
      "s_waitcnt lgkmcnt(1)\n\t"
      "v_pk_add_f32 v[20:21], v[20:21], v[232:233]\n\t"
      "v_pk_add_f32 v[22:23], v[22:23], v[234:235]\n\t"
      "v_mfma_f32_16x16x32_bf16 v[144:147], a[196:199], a[148:151], v[144:147]\n\t"
      "v_exp_f32 v139, v139\n\t"
      "v_pk_add_f32 v[24:25], v[24:25], v[236:237]\n\t"
      "v_pk_add_f32 v[26:27], v[26:27], v[238:239]\n\t"
      "v_pk_mul_f32 v[20:21], v[20:21], v[220:221] op_sel:[0,1] op_sel_hi:[1,1]\n\t"
      "v_mfma_f32_16x16x32_bf16 v[144:147], a[192:195], a[144:147], v[144:147]\n\t"
      "v_cvt_pk_bf16_f32 v168, v132, v133\n\t"
      "v_cvt_pk_bf16_f32 v169, v134, v135\n\t"
      "v_pk_mul_f32 v[22:23], v[22:23], v[220:221] op_sel:[0,1] op_sel_hi:[1,1]\n\t"
      "v_pk_mul_f32 v[24:25], v[24:25], v[220:221] op_sel:[0,1] op_sel_hi:[1,1]\n\t"
      "v_pk_mul_f32 v[26:27], v[26:27], v[220:221] op_sel:[0,1] op_sel_hi:[1,1]\n\t"
      "v_mfma_f32_16x16x32_bf16 v[148:151], a[204:207], a[144:147], v[200:203]\n\t"
      "v_exp_f32 v140, v140\n\t"
      "v_cvt_pk_bf16_f32 v20, v20, v21\n\t"
      "v_cvt_pk_bf16_f32 v21, v22, v23\n\t"
      "v_cvt_pk_bf16_f32 v22, v24, v25\n\t"
      "v_cvt_pk_bf16_f32 v23, v26, v27\n\t"
      "v_cndmask_b32 v16, v4, v20, vcc\n\t"
      "v_mfma_f32_16x16x32_bf16 v[148:151], a[208:211], a[148:151], v[148:151]\n\t"
      "v_exp_f32 v141, v141\n\t"
      "v_cndmask_b32 v17, v5, v21, vcc\n\t"
      "v_cndmask_b32 v18, v6, v22, vcc\n\t"
      "v_cndmask_b32 v19, v7, v23, vcc\n\t"
      "global_store_dwordx4 %1, v[16:19], s[22:23] offset:128 sc1\n\t"
      "v_mfma_f32_16x16x32_bf16 v[148:151], a[212:215], a[152:155], v[148:151]\n\t"
      "v_exp_f32 v142, v142\n\t"
      "s_not_b64 vcc, vcc\n\t"
      "v_cndmask_b32_dpp v24, v12, v20, vcc quad_perm:[1,0,3,2] row_mask:0xf bank_mask:0xf\n\t"
      "v_cndmask_b32_dpp v25, v13, v21, vcc quad_perm:[1,0,3,2] row_mask:0xf bank_mask:0xf\n\t"
      "v_cndmask_b32_dpp v26, v14, v22, vcc quad_perm:[1,0,3,2] row_mask:0xf bank_mask:0xf\n\t"
      "v_cndmask_b32_dpp v27, v15, v23, vcc quad_perm:[1,0,3,2] row_mask:0xf bank_mask:0xf\n\t"
      "v_mfma_f32_16x16x32_bf16 v[152:155], a[224:227], a[152:155], v[200:203]\n\t"
      "v_exp_f32 v143, v143\n\t"
      "global_store_dwordx4 %1, v[24:27], s[22:23] offset:256 sc1\n\t"
      "v_mfma_f32_16x16x32_bf16 v[152:155], a[220:223], a[148:151], v[152:155]\n\t"
      "v_cvt_pk_bf16_f32 v170, v136, v137\n\t"
      "v_cvt_pk_bf16_f32 v171, v138, v139\n\t"
      "v_mfma_f32_16x16x32_bf16 v[152:155], a[216:219], a[144:147], v[152:155]\n\t"
      "v_exp_f32 v144, v144\n\t"
      "v_mfma_f32_16x16x32_bf16 a[252:255], v[212:215], v[180:183], a[252:255]\n\t"
      "v_exp_f32 v145, v145\n\t"
      "v_mfma_f32_16x16x32_bf16 a[72:75], v[76:79], v[180:183], a[72:75]\n\t"
      "v_exp_f32 v146, v146\n\t"
      "v_mfma_f32_16x16x32_bf16 a[76:79], v[80:83], v[180:183], a[76:79]\n\t"
      "v_exp_f32 v147, v147\n\t"
      "v_mfma_f32_16x16x32_bf16 a[80:83], v[84:87], v[180:183], a[80:83]\n\t"
      "v_cvt_pk_bf16_f32 v172, v140, v141\n\t"
      "v_cvt_pk_bf16_f32 v173, v142, v143\n\t"
      "v_accvgpr_read_b32 v249, a252\n\t"
      "ds_write_b32 v244, v249 offset:384\n\t"
      "ds_write_b128 v242, a[72:75] offset:0\n\t"
      "v_mfma_f32_16x16x32_bf16 a[84:87], v[88:91], v[180:183], a[84:87]\n\t"
      "v_exp_f32 v148, v148\n\t"
      "v_mfma_f32_16x16x32_bf16 a[88:91], v[92:95], v[180:183], a[88:91]\n\t"
      "v_exp_f32 v149, v149\n\t"
      "ds_write_b128 v242, a[76:79] offset:1024\n\t"
      "v_mfma_f32_16x16x32_bf16 a[92:95], v[96:99], v[180:183], a[92:95]\n\t"
      "v_exp_f32 v150, v150\n\t"
      "v_mfma_f32_16x16x32_bf16 v[124:127], a[180:183], a[120:123], v[192:195]\n\t"
      "v_exp_f32 v151, v151\n\t"
      "ds_write_b128 v242, a[80:83] offset:2048\n\t"
      "v_mfma_f32_16x16x32_bf16 v[124:127], a[184:187], a[124:127], v[124:127]\n\t"
      "v_cvt_pk_bf16_f32 v174, v144, v145\n\t"
      "v_cvt_pk_bf16_f32 v175, v146, v147\n\t"
      "v_mfma_f32_16x16x32_bf16 v[124:127], a[188:191], a[128:131], v[124:127]\n\t"
      "v_exp_f32 v152, v152\n\t"
      "ds_write_b128 v242, a[84:87] offset:3072\n\t"
      "v_mfma_f32_16x16x32_bf16 v[128:131], a[200:203], a[128:131], v[192:195]\n\t"
      "v_exp_f32 v153, v153\n\t"
      "v_mfma_f32_16x16x32_bf16 v[128:131], a[196:199], a[124:127], v[128:131]\n\t"
      "v_exp_f32 v154, v154\n\t"
      "ds_write_b128 v242, a[88:91] offset:4096\n\t"
      "v_mfma_f32_16x16x32_bf16 v[128:131], a[192:195], a[120:123], v[128:131]\n\t"
      "v_exp_f32 v155, v155\n\t"
      "v_mfma_f32_16x16x32_bf16 v[132:135], a[204:207], a[120:123], v[192:195]\n\t"
      "v_cvt_pk_bf16_f32 v176, v148, v149\n\t"
      "v_cvt_pk_bf16_f32 v177, v150, v151\n\t"
      "ds_write_b128 v242, a[92:95] offset:5120\n\t"
      "v_mfma_f32_16x16x32_bf16 v[132:135], a[208:211], a[124:127], v[132:135]\n\t"
      "v_exp_f32 v124, v124\n\t"
      "v_mfma_f32_16x16x32_bf16 v[132:135], a[212:215], a[128:131], v[132:135]\n\t"
      "v_exp_f32 v125, v125\n\t"
      "v_mfma_f32_16x16x32_bf16 v[136:139], a[224:227], a[128:131], v[192:195]\n\t"
      "v_exp_f32 v126, v126\n\t"
      "v_mfma_f32_16x16x32_bf16 v[136:139], a[220:223], a[124:127], v[136:139]\n\t"
      "v_exp_f32 v127, v127\n\t"
      "v_mfma_f32_16x16x32_bf16 v[136:139], a[216:219], a[120:123], v[136:139]\n\t"
      "v_exp_f32 v128, v128\n\t"
      "v_smfmac_f32_16x16x64_bf16 a[232:235], v[212:215], v[164:171], v216\n\t"
      "v_exp_f32 v129, v129\n\t"
      "v_mfma_f32_16x16x32_bf16 a[24:27], v[100:103], v[168:171], a[24:27]\n\t"
      "v_exp_f32 v130, v130\n\t"
      "v_mfma_f32_16x16x32_bf16 a[24:27], v[76:79], v[164:167], a[24:27]\n\t"
      "v_exp_f32 v131, v131\n\t"
      "v_mfma_f32_16x16x32_bf16 a[28:31], v[80:83], v[164:167], a[28:31]\n\t"
      "v_exp_f32 v132, v132\n\t"
      "v_mfma_f32_16x16x32_bf16 a[28:31], v[104:107], v[168:171], a[28:31]\n\t"
      "v_exp_f32 v133, v133\n\t"
      "v_accvgpr_read_b32 v249, a232\n\t"
      "v_accvgpr_read_b32 v250, a233\n\t"
      "v_add_f32 v249, v249, v250\n\t"
      "ds_write_b32 v244, v249 offset:64\n\t"
      "ds_write_b128 v240, a[24:27] offset:6144\n\t"
      "v_mfma_f32_16x16x32_bf16 a[32:35], v[108:111], v[168:171], a[32:35]\n\t"
      "v_exp_f32 v134, v134\n\t"
      "v_mfma_f32_16x16x32_bf16 a[32:35], v[84:87], v[164:167], a[32:35]\n\t"
      "v_exp_f32 v135, v135\n\t"
      "ds_write_b128 v240, a[28:31] offset:7168\n\t"
      "v_mfma_f32_16x16x32_bf16 a[36:39], v[88:91], v[164:167], a[36:39]\n\t"
      "v_exp_f32 v136, v136\n\t"
      "v_mfma_f32_16x16x32_bf16 a[36:39], v[112:115], v[168:171], a[36:39]\n\t"
      "v_exp_f32 v137, v137\n\t"
      "v_mfma_f32_16x16x32_bf16 a[40:43], v[116:119], v[168:171], a[40:43]\n\t"
      "v_exp_f32 v138, v138\n\t"
      "ds_write_b128 v240, a[32:35] offset:8192\n\t"
      "v_mfma_f32_16x16x32_bf16 a[40:43], v[92:95], v[164:167], a[40:43]\n\t"
      "v_exp_f32 v139, v139\n\t"
      "v_mfma_f32_16x16x32_bf16 a[44:47], v[96:99], v[164:167], a[44:47]\n\t"
      "v_cvt_pk_bf16_f32 v178, v152, v153\n\t"
      "v_cvt_pk_bf16_f32 v179, v154, v155\n\t"
      "ds_write_b128 v240, a[36:39] offset:9216\n\t"
      "v_mfma_f32_16x16x32_bf16 a[44:47], v[120:123], v[168:171], a[44:47]\n\t"
      "v_cvt_pk_bf16_f32 v164, v124, v125\n\t"
      "v_cvt_pk_bf16_f32 v165, v126, v127\n\t"
      "v_mfma_f32_16x16x32_bf16 v[140:143], a[180:183], a[156:159], v[204:207]\n\t"
      "v_cvt_pk_bf16_f32 v166, v128, v129\n\t"
      "v_cvt_pk_bf16_f32 v167, v130, v131\n\t"
      "ds_write_b128 v240, a[40:43] offset:10240\n\t"
      "v_mfma_f32_16x16x32_bf16 v[140:143], a[184:187], a[160:163], v[140:143]\n\t"
      "v_cvt_pk_bf16_f32 v168, v132, v133\n\t"
      "v_cvt_pk_bf16_f32 v169, v134, v135\n\t"
      "v_mfma_f32_16x16x32_bf16 v[140:143], a[188:191], a[164:167], v[140:143]\n\t"
      "v_mfma_f32_16x16x32_bf16 v[144:147], a[200:203], a[164:167], v[204:207]\n\t"
      "ds_write_b128 v240, a[44:47] offset:11264\n\t"
      "v_mfma_f32_16x16x32_bf16 v[144:147], a[196:199], a[160:163], v[144:147]\n\t"
      "v_cvt_pk_bf16_f32 v170, v136, v137\n\t"
      "v_cvt_pk_bf16_f32 v171, v138, v139\n\t"
      "v_mfma_f32_16x16x32_bf16 v[144:147], a[192:195], a[156:159], v[144:147]\n\t"
      "v_mfma_f32_16x16x32_bf16 v[148:151], a[204:207], a[156:159], v[204:207]\n\t"
      "s_nop 0\n\t"
      "v_exp_f32 v140, v140\n\t"
      "v_mfma_f32_16x16x32_bf16 v[148:151], a[208:211], a[160:163], v[148:151]\n\t"
      "v_exp_f32 v141, v141\n\t"
      "v_mfma_f32_16x16x32_bf16 v[148:151], a[212:215], a[164:167], v[148:151]\n\t"
      "s_waitcnt lgkmcnt(0)\n\t"
      "s_barrier\n\t"
      "ds_read_b32 v222, v245 offset:64\n\t"
      "ds_read_b32 v180, v246 offset:0\n\t"
      "ds_read_b32 v181, v246 offset:448\n\t"
      "ds_read_b32 v182, v246 offset:896\n\t"
      "ds_read_b32 v183, v246 offset:1344\n\t"
      "v_mfma_f32_16x16x32_bf16 v[152:155], a[224:227], a[164:167], v[204:207]\n\t"
      "v_exp_f32 v142, v142\n\t"
      "ds_read_b128 v[156:159], v243 offset:0\n\t"
      "ds_read_b128 v[160:163], v243 offset:1024\n\t"
      "ds_read_b128 v[184:187], v243 offset:6144\n\t"
      "ds_read_b128 v[196:199], v243 offset:7168\n\t"
      "ds_read_b128 v[224:227], v243 offset:12288\n\t"
      "v_mfma_f32_16x16x32_bf16 v[152:155], a[220:223], a[160:163], v[152:155]\n\t"
      "v_exp_f32 v143, v143\n\t"
      "ds_read_b128 v[228:231], v243 offset:13312\n\t"
      "ds_read_b128 v[232:235], v243 offset:18432\n\t"
      "ds_read_b128 v[236:239], v243 offset:19456\n\t"
      "v_mfma_f32_16x16x32_bf16 v[152:155], a[216:219], a[156:159], v[152:155]\n\t"
      "v_smfmac_f32_16x16x64_bf16 a[244:247], v[212:215], v[172:179], v216\n\t"
      "v_exp_f32 v144, v144\n\t"
      "v_mfma_f32_16x16x32_bf16 v[28:31], v[100:103], v[176:179], v[28:31]\n\t"
      "v_mfma_f32_16x16x32_bf16 v[28:31], v[76:79], v[172:175], v[28:31]\n\t"
      "v_exp_f32 v145, v145\n\t"
      "s_waitcnt lgkmcnt(8)\n\t"
      "v_add_f32 v180, v180, v181\n\t"
      "v_add_f32 v182, v182, v183\n\t"
      "v_add_f32 v180, v180, v182\n\t"
      "v_subrev_u32 v220, 0.5, v180\n\t"
      "v_mfma_f32_16x16x32_bf16 v[32:35], v[80:83], v[172:175], v[32:35]\n\t"
      "v_max_u32 v217, v217, v220\n\t"
      "v_rcp_f32 v180, v180\n\t"
      "s_waitcnt lgkmcnt(4)\n\t"
      "v_pk_add_f32 v[156:157], v[156:157], v[184:185]\n\t"
      "v_pk_add_f32 v[158:159], v[158:159], v[186:187]\n\t"
      "v_mfma_f32_16x16x32_bf16 v[32:35], v[104:107], v[176:179], v[32:35]\n\t"
      "v_exp_f32 v146, v146\n\t"
      "ds_read_b128 v[184:187], v241 offset:6144\n\t"
      "v_pk_add_f32 v[160:161], v[160:161], v[196:197]\n\t"
      "v_pk_add_f32 v[162:163], v[162:163], v[198:199]\n\t"
      "v_mfma_f32_16x16x32_bf16 v[36:39], v[108:111], v[176:179], v[36:39]\n\t"
      "v_exp_f32 v147, v147\n\t"
      "ds_read_b128 v[196:199], v241 offset:7168\n\t"
      "s_waitcnt lgkmcnt(4)\n\t"
      "v_pk_add_f32 v[156:157], v[156:157], v[224:225]\n\t"
      "v_pk_add_f32 v[158:159], v[158:159], v[226:227]\n\t"
      "v_mfma_f32_16x16x32_bf16 v[36:39], v[84:87], v[172:175], v[36:39]\n\t"
      "v_exp_f32 v148, v148\n\t"
      "ds_read_b128 v[224:227], v241 offset:8192\n\t"
      "v_pk_add_f32 v[160:161], v[160:161], v[228:229]\n\t"
      "v_pk_add_f32 v[162:163], v[162:163], v[230:231]\n\t"
      "v_mfma_f32_16x16x32_bf16 v[40:43], v[88:91], v[172:175], v[40:43]\n\t"
      "v_exp_f32 v149, v149\n\t"
      "ds_read_b128 v[228:231], v241 offset:9216\n\t"
      "s_waitcnt lgkmcnt(4)\n\t"
      "v_pk_add_f32 v[156:157], v[156:157], v[232:233]\n\t"
      "v_pk_add_f32 v[158:159], v[158:159], v[234:235]\n\t"
      "v_mfma_f32_16x16x32_bf16 v[40:43], v[112:115], v[176:179], v[40:43]\n\t"
      "v_exp_f32 v150, v150\n\t"
      "ds_read_b128 v[232:235], v241 offset:10240\n\t"
      "v_pk_add_f32 v[160:161], v[160:161], v[236:237]\n\t"
      "v_pk_add_f32 v[162:163], v[162:163], v[238:239]\n\t"
      "v_mfma_f32_16x16x32_bf16 v[44:47], v[116:119], v[176:179], v[44:47]\n\t"
      "ds_read_b128 v[236:239], v241 offset:11264\n\t"
      "v_pk_mul_f32 v[156:157], v[156:157], v[180:181] op_sel_hi:[1,0]\n\t"
      "v_pk_mul_f32 v[158:159], v[158:159], v[180:181] op_sel_hi:[1,0]\n\t"
      "v_pk_mul_f32 v[160:161], v[160:161], v[180:181] op_sel_hi:[1,0]\n\t"
      "v_mfma_f32_16x16x32_bf16 v[44:47], v[92:95], v[172:175], v[44:47]\n\t"
      "v_pk_mul_f32 v[162:163], v[162:163], v[180:181] op_sel_hi:[1,0]\n\t"
      "v_cvt_pk_bf16_f32 v156, v156, v157\n\t"
      "v_cvt_pk_bf16_f32 v157, v158, v159\n\t"
      "v_cvt_pk_bf16_f32 v158, v160, v161\n\t"
      "v_cvt_pk_bf16_f32 v159, v162, v163\n\t"
      "s_mov_b64 exec, s[38:39]\n\t"
      "global_store_dwordx4 v248, v[156:159], s[22:23] sc1\n\t"
      "s_mov_b64 exec, -1\n\t"
      "v_mfma_f32_16x16x32_bf16 v[48:51], v[96:99], v[172:175], v[48:51]\n\t"
      "v_exp_f32 v151, v151\n\t"
      "v_accvgpr_read_b32 v219, a244\n\t"
      "v_accvgpr_read_b32 v220, a245\n\t"
      "v_add_f32 v219, v219, v220\n\t"
      "ds_bpermute_b32 v218, v247, v219\n\t"
      "s_waitcnt lgkmcnt(5)\n\t"
      "v_mfma_f32_16x16x32_bf16 v[48:51], v[120:123], v[176:179], v[48:51]\n\t"
      "v_cvt_pk_bf16_f32 v172, v140, v141\n\t"
      "v_cvt_pk_bf16_f32 v173, v142, v143\n\t"
      "v_cvt_pk_bf16_f32 v174, v144, v145\n\t"
      "v_exp_f32 v152, v152\n\t"
      "v_exp_f32 v153, v153\n\t"
      "v_cvt_pk_bf16_f32 v175, v146, v147\n\t"
      "v_cvt_pk_bf16_f32 v176, v148, v149\n\t"
      "v_cvt_pk_bf16_f32 v177, v150, v151\n\t"
      "v_exp_f32 v154, v154\n\t"
      "v_exp_f32 v155, v155\n\t"
      "v_smfmac_f32_16x16x64_bf16 a[236:239], v[212:215], v[164:171], v216\n\t"
      "v_pk_add_f32 v[28:29], v[28:29], v[184:185]\n\t"
      "v_pk_add_f32 v[30:31], v[30:31], v[186:187]\n\t"
      "v_pk_add_f32 v[32:33], v[32:33], v[196:197]\n\t"
      "v_pk_add_f32 v[34:35], v[34:35], v[198:199]\n\t"
      "v_mfma_f32_16x16x32_bf16 a[48:51], v[100:103], v[168:171], a[48:51]\n\t"
      "s_waitcnt lgkmcnt(0)\n\t"
      "v_add_f32 v222, v222, v218\n\t"
      "v_subrev_u32 v220, 0.5, v222\n\t"
      "v_accvgpr_read_b32 v249, a236\n\t"
      "v_accvgpr_read_b32 v250, a237\n\t"
      "v_add_f32 v249, v249, v250\n\t"
      "ds_write_b32 v244, v249 offset:128\n\t"
      "v_mfma_f32_16x16x32_bf16 a[48:51], v[76:79], v[164:167], a[48:51]\n\t"
      "v_max_u32 v217, v217, v220\n\t"
      "v_rcp_f32 v222, v222\n\t"
      "s_nop 0\n\t"
      "v_pk_mul_f32 v[28:29], v[28:29], v[222:223] op_sel_hi:[1,0]\n\t"
      "v_pk_mul_f32 v[30:31], v[30:31], v[222:223] op_sel_hi:[1,0]\n\t"
      "v_mfma_f32_16x16x32_bf16 a[52:55], v[80:83], v[164:167], a[52:55]\n\t"
      "v_pk_mul_f32 v[32:33], v[32:33], v[222:223] op_sel_hi:[1,0]\n\t"
      "v_pk_mul_f32 v[34:35], v[34:35], v[222:223] op_sel_hi:[1,0]\n\t"
      "v_cvt_pk_bf16_f32 v28, v28, v29\n\t"
      "ds_write_b128 v240, a[48:51] offset:12288\n\t"
      "v_mfma_f32_16x16x32_bf16 a[52:55], v[104:107], v[168:171], a[52:55]\n\t"
      "v_cvt_pk_bf16_f32 v29, v30, v31\n\t"
      "v_cvt_pk_bf16_f32 v30, v32, v33\n\t"
      "v_cvt_pk_bf16_f32 v31, v34, v35\n\t"
      "s_waitcnt lgkmcnt(5)\n\t"
      "v_pk_add_f32 v[36:37], v[36:37], v[224:225]\n\t"
      "v_pk_add_f32 v[38:39], v[38:39], v[226:227]\n\t"
      "v_mfma_f32_16x16x32_bf16 a[56:59], v[108:111], v[168:171], a[56:59]\n\t"
      "v_pk_add_f32 v[40:41], v[40:41], v[228:229]\n\t"
      "ds_write_b128 v240, a[52:55] offset:13312\n\t"
      "v_pk_add_f32 v[42:43], v[42:43], v[230:231]\n\t"
      "v_mfma_f32_16x16x32_bf16 a[56:59], v[84:87], v[164:167], a[56:59]\n\t"
      "v_pk_mul_f32 v[36:37], v[36:37], v[222:223] op_sel_hi:[1,0]\n\t"
      "v_pk_mul_f32 v[38:39], v[38:39], v[222:223] op_sel_hi:[1,0]\n\t"
      "v_pk_mul_f32 v[40:41], v[40:41], v[222:223] op_sel_hi:[1,0]\n\t"
      "v_pk_mul_f32 v[42:43], v[42:43], v[222:223] op_sel_hi:[1,0]\n\t"
      "v_mfma_f32_16x16x32_bf16 a[60:63], v[88:91], v[164:167], a[60:63]\n\t"
      "v_cvt_pk_bf16_f32 v36, v36, v37\n\t"
      "v_cvt_pk_bf16_f32 v37, v38, v39\n\t"
      "v_cvt_pk_bf16_f32 v38, v40, v41\n\t"
      "ds_write_b128 v240, a[56:59] offset:14336\n\t"
      "v_mfma_f32_16x16x32_bf16 a[60:63], v[112:115], v[168:171], a[60:63]\n\t"
      "v_cvt_pk_bf16_f32 v39, v42, v43\n\t"
      "s_mov_b64 vcc, s[36:37]\n\t"
      "v_cndmask_b32_dpp v32, v36, v28, vcc quad_perm:[1,0,3,2] row_mask:0xf bank_mask:0xf\n\t"
      "v_cndmask_b32_dpp v33, v37, v29, vcc quad_perm:[1,0,3,2] row_mask:0xf bank_mask:0xf\n\t"
      "v_cndmask_b32_dpp v34, v38, v30, vcc quad_perm:[1,0,3,2] row_mask:0xf bank_mask:0xf\n\t"
      "v_cndmask_b32_dpp v35, v39, v31, vcc quad_perm:[1,0,3,2] row_mask:0xf bank_mask:0xf\n\t"
      "global_store_dwordx4 %2, v[32:35], s[22:23] offset:0 sc1\n\t"
      "v_mfma_f32_16x16x32_bf16 a[64:67], v[116:119], v[168:171], a[64:67]\n\t"
      "ds_write_b128 v240, a[60:63] offset:15360\n\t"
      "s_waitcnt lgkmcnt(6)\n\t"
      "v_pk_add_f32 v[44:45], v[44:45], v[232:233]\n\t"
      "v_mfma_f32_16x16x32_bf16 a[64:67], v[92:95], v[164:167], a[64:67]\n\t"
      "v_pk_add_f32 v[46:47], v[46:47], v[234:235]\n\t"
      "v_pk_add_f32 v[48:49], v[48:49], v[236:237]\n\t"
      "v_pk_add_f32 v[50:51], v[50:51], v[238:239]\n\t"
      "v_pk_mul_f32 v[44:45], v[44:45], v[222:223] op_sel_hi:[1,0]\n\t"
      "v_mfma_f32_16x16x32_bf16 a[68:71], v[96:99], v[164:167], a[68:71]\n\t"
      "v_pk_mul_f32 v[46:47], v[46:47], v[222:223] op_sel_hi:[1,0]\n\t"
      "v_pk_mul_f32 v[48:49], v[48:49], v[222:223] op_sel_hi:[1,0]\n\t"
      "v_pk_mul_f32 v[50:51], v[50:51], v[222:223] op_sel_hi:[1,0]\n\t"
      "ds_write_b128 v240, a[64:67] offset:16384\n\t"
      "v_mfma_f32_16x16x32_bf16 a[68:71], v[120:123], v[168:171], a[68:71]\n\t"
      "v_cvt_pk_bf16_f32 v178, v152, v153\n\t"
      "v_cvt_pk_bf16_f32 v179, v154, v155\n\t"
      "v_cvt_pk_bf16_f32 v44, v44, v45\n\t"
      "v_cvt_pk_bf16_f32 v45, v46, v47\n\t"
      "v_cvt_pk_bf16_f32 v46, v48, v49\n\t"
      "v_cvt_pk_bf16_f32 v47, v50, v51\n\t"
      "v_cndmask_b32 v40, v28, v44, vcc\n\t"
      "v_smfmac_f32_16x16x64_bf16 a[248:251], v[212:215], v[172:179], v216\n\t"
      "ds_write_b128 v240, a[68:71] offset:17408\n\t"
      "v_cndmask_b32 v41, v29, v45, vcc\n\t"
      "v_cndmask_b32 v42, v30, v46, vcc\n\t"
      "v_cndmask_b32 v43, v31, v47, vcc\n\t"
      "v_mfma_f32_16x16x32_bf16 v[52:55], v[100:103], v[176:179], v[52:55]\n\t"
      "global_store_dwordx4 %2, v[40:43], s[22:23] offset:128 sc1\n\t"
      "s_not_b64 vcc, vcc\n\t"
      "v_cndmask_b32_dpp v48, v36, v44, vcc quad_perm:[1,0,3,2] row_mask:0xf bank_mask:0xf\n\t"
      "v_cndmask_b32_dpp v49, v37, v45, vcc quad_perm:[1,0,3,2] row_mask:0xf bank_mask:0xf\n\t"
      "v_mfma_f32_16x16x32_bf16 v[52:55], v[76:79], v[172:175], v[52:55]\n\t"
      "v_cndmask_b32_dpp v50, v38, v46, vcc quad_perm:[1,0,3,2] row_mask:0xf bank_mask:0xf\n\t"
      "v_cndmask_b32_dpp v51, v39, v47, vcc quad_perm:[1,0,3,2] row_mask:0xf bank_mask:0xf\n\t"
      "global_store_dwordx4 %2, v[48:51], s[22:23] offset:256 sc1\n\t"
      "s_waitcnt lgkmcnt(0)\n\t"
      "s_barrier\n\t"
      "v_mfma_f32_16x16x32_bf16 v[56:59], v[80:83], v[172:175], v[56:59]\n\t"
      "ds_read_b32 v221, v245 offset:128\n\t"
      "ds_read_b128 v[184:187], v241 offset:12288\n\t"
      "ds_read_b128 v[196:199], v241 offset:13312\n\t"
      "ds_read_b128 v[224:227], v241 offset:14336\n\t"
      "ds_read_b128 v[228:231], v241 offset:15360\n\t"
      "ds_read_b128 v[232:235], v241 offset:16384\n\t"
      "ds_read_b128 v[236:239], v241 offset:17408\n\t"
      "v_mfma_f32_16x16x32_bf16 v[56:59], v[104:107], v[176:179], v[56:59]\n\t"
      "v_accvgpr_read_b32 v219, a248\n\t"
      "v_accvgpr_read_b32 v220, a249\n\t"
      "v_add_f32 v219, v219, v220\n\t"
      "ds_bpermute_b32 v218, v247, v219\n\t"
      "v_mfma_f32_16x16x32_bf16 v[60:63], v[108:111], v[176:179], v[60:63]\n\t"
      "v_mfma_f32_16x16x32_bf16 v[60:63], v[84:87], v[172:175], v[60:63]\n\t"
      "v_mfma_f32_16x16x32_bf16 v[64:67], v[88:91], v[172:175], v[64:67]\n\t"
      "v_mfma_f32_16x16x32_bf16 v[64:67], v[112:115], v[176:179], v[64:67]\n\t"
      "v_mfma_f32_16x16x32_bf16 v[68:71], v[116:119], v[176:179], v[68:71]\n\t"
      "s_waitcnt lgkmcnt(5)\n\t"
      "v_pk_add_f32 v[52:53], v[52:53], v[184:185]\n\t"
      "v_pk_add_f32 v[54:55], v[54:55], v[186:187]\n\t"
      "v_pk_add_f32 v[56:57], v[56:57], v[196:197]\n\t"
      "v_mfma_f32_16x16x32_bf16 v[68:71], v[92:95], v[172:175], v[68:71]\n\t"
      "v_pk_add_f32 v[58:59], v[58:59], v[198:199]\n\t"
      "s_waitcnt lgkmcnt(0)\n\t"
      "v_add_f32 v221, v221, v218\n\t"
      "v_subrev_u32 v220, 0.5, v221\n\t"
      "v_max_u32 v217, v217, v220\n\t"
      "v_rcp_f32 v221, v221\n\t"
      "v_mfma_f32_16x16x32_bf16 v[72:75], v[96:99], v[172:175], v[72:75]\n\t"
      "v_pk_mul_f32 v[52:53], v[52:53], v[220:221] op_sel:[0,1] op_sel_hi:[1,1]\n\t"
      "v_pk_mul_f32 v[54:55], v[54:55], v[220:221] op_sel:[0,1] op_sel_hi:[1,1]\n\t"
      "v_pk_mul_f32 v[56:57], v[56:57], v[220:221] op_sel:[0,1] op_sel_hi:[1,1]\n\t"
      "v_pk_mul_f32 v[58:59], v[58:59], v[220:221] op_sel:[0,1] op_sel_hi:[1,1]\n\t"
      "v_mfma_f32_16x16x32_bf16 v[72:75], v[120:123], v[176:179], v[72:75]\n\t"
      "v_cvt_pk_bf16_f32 v52, v52, v53\n\t"
      "v_cvt_pk_bf16_f32 v53, v54, v55\n\t"
      "v_cvt_pk_bf16_f32 v54, v56, v57\n\t"
      "v_cvt_pk_bf16_f32 v55, v58, v59\n\t"
      "s_waitcnt lgkmcnt(3)\n\t"
      "v_pk_add_f32 v[60:61], v[60:61], v[224:225]\n\t"
      "v_pk_add_f32 v[62:63], v[62:63], v[226:227]\n\t"
      "v_pk_add_f32 v[64:65], v[64:65], v[228:229]\n\t"
      "v_pk_add_f32 v[66:67], v[66:67], v[230:231]\n\t"
      "v_pk_mul_f32 v[60:61], v[60:61], v[220:221] op_sel:[0,1] op_sel_hi:[1,1]\n\t"
      "v_pk_mul_f32 v[62:63], v[62:63], v[220:221] op_sel:[0,1] op_sel_hi:[1,1]\n\t"
      "v_pk_mul_f32 v[64:65], v[64:65], v[220:221] op_sel:[0,1] op_sel_hi:[1,1]\n\t"
      "v_pk_mul_f32 v[66:67], v[66:67], v[220:221] op_sel:[0,1] op_sel_hi:[1,1]\n\t"
      "v_cvt_pk_bf16_f32 v60, v60, v61\n\t"
      "v_cvt_pk_bf16_f32 v61, v62, v63\n\t"
      "v_cvt_pk_bf16_f32 v62, v64, v65\n\t"
      "v_cvt_pk_bf16_f32 v63, v66, v67\n\t"
      "s_mov_b64 vcc, s[36:37]\n\t"
      "v_cndmask_b32_dpp v56, v60, v52, vcc quad_perm:[1,0,3,2] row_mask:0xf bank_mask:0xf\n\t"
      "v_cndmask_b32_dpp v57, v61, v53, vcc quad_perm:[1,0,3,2] row_mask:0xf bank_mask:0xf\n\t"
      "v_cndmask_b32_dpp v58, v62, v54, vcc quad_perm:[1,0,3,2] row_mask:0xf bank_mask:0xf\n\t"
      "v_cndmask_b32_dpp v59, v63, v55, vcc quad_perm:[1,0,3,2] row_mask:0xf bank_mask:0xf\n\t"
      "global_store_dwordx4 %3, v[56:59], s[22:23] offset:0 sc1\n\t"
      "s_waitcnt lgkmcnt(1)\n\t"
      "v_pk_add_f32 v[68:69], v[68:69], v[232:233]\n\t"
      "v_pk_add_f32 v[70:71], v[70:71], v[234:235]\n\t"
      "v_pk_add_f32 v[72:73], v[72:73], v[236:237]\n\t"
      "v_pk_add_f32 v[74:75], v[74:75], v[238:239]\n\t"
      "v_pk_mul_f32 v[68:69], v[68:69], v[220:221] op_sel:[0,1] op_sel_hi:[1,1]\n\t"
      "v_pk_mul_f32 v[70:71], v[70:71], v[220:221] op_sel:[0,1] op_sel_hi:[1,1]\n\t"
      "v_pk_mul_f32 v[72:73], v[72:73], v[220:221] op_sel:[0,1] op_sel_hi:[1,1]\n\t"
      "v_pk_mul_f32 v[74:75], v[74:75], v[220:221] op_sel:[0,1] op_sel_hi:[1,1]\n\t"
      "v_cvt_pk_bf16_f32 v68, v68, v69\n\t"
      "v_cvt_pk_bf16_f32 v69, v70, v71\n\t"
      "v_cvt_pk_bf16_f32 v70, v72, v73\n\t"
      "v_cvt_pk_bf16_f32 v71, v74, v75\n\t"
      "v_cndmask_b32 v64, v52, v68, vcc\n\t"
      "v_cndmask_b32 v65, v53, v69, vcc\n\t"
      "v_cndmask_b32 v66, v54, v70, vcc\n\t"
      "v_cndmask_b32 v67, v55, v71, vcc\n\t"
      "global_store_dwordx4 %3, v[64:67], s[22:23] offset:128 sc1\n\t"
      "s_not_b64 vcc, vcc\n\t"
      "v_cndmask_b32_dpp v72, v60, v68, vcc quad_perm:[1,0,3,2] row_mask:0xf bank_mask:0xf\n\t"
      "v_cndmask_b32_dpp v73, v61, v69, vcc quad_perm:[1,0,3,2] row_mask:0xf bank_mask:0xf\n\t"
      "v_cndmask_b32_dpp v74, v62, v70, vcc quad_perm:[1,0,3,2] row_mask:0xf bank_mask:0xf\n\t"
      "v_cndmask_b32_dpp v75, v63, v71, vcc quad_perm:[1,0,3,2] row_mask:0xf bank_mask:0xf\n\t"
      "global_store_dwordx4 %3, v[72:75], s[22:23] offset:256 sc1\n\t"
      "s_mov_b32 s48, 847249408\n\t"
      "s_waitcnt vmcnt(9)\n\t"
      "v_cmp_ne_u32 vcc, s54, v223\n\t"
      "v_cmp_gt_u32 s[52:53], s48, v217\n\t"
      "s_nop 0\n\t"
      "s_and_b64 s[52:53], s[52:53], vcc\n\t"
      "Lfinish%=:\n\t"
      "s_cmp_lg_u32 s45, 3\n\t"
      "s_cbranch_scc1 Lmain_done%=\n\t"
      "s_cmp_lg_u32 %6, 0\n\t"
      "s_cbranch_scc1 Lmain_done%=\n\t"
      "v_mov_b32 v148, 524288\n\t"
      "v_mov_b32 v149, s54\n\t"
      "s_mov_b64 exec, 1\n\t"
      "global_store_dword v148, v149, s[40:41] sc1\n\t"
      "s_mov_b64 exec, -1\n\t"
      "Lmain_done%=:\n\t"
      "s_cmp_eq_u64 s[52:53], -1\n\t"
      "s_cselect_b32 %0, 0, 1\n\t"
      "Lend%=:\n\t"
      : "=s"(bad), "=&v"(x3), "=&v"(x4), "=&v"(x5), "=&v"(x6)
      : "s"(__builtin_amdgcn_kernarg_segment_ptr()), "s"(b), "v"(tid)
      : "v4", "v5", "v6", "v7", "v8", "v9", "v10", "v11", "v12", "v13", "v14", "v15", "v16", "v17", "v18", "v19", "v20", "v21", "v22", "v23", "v24", "v25", "v26", "v27", "v28", "v29", "v30", "v31", "v32", "v33", "v34", "v35", "v36", "v37", "v38", "v39", "v40", "v41", "v42", "v43", "v44", "v45", "v46", "v47", "v48", "v49", "v50", "v51", "v52", "v53", "v54", "v55", "v56", "v57", "v58", "v59", "v60", "v61", "v62", "v63", "v64", "v65", "v66", "v67", "v68", "v69", "v70", "v71", "v72", "v73", "v74", "v75", "v76", "v77", "v78", "v79", "v80", "v81", "v82", "v83", "v84", "v85", "v86", "v87", "v88", "v89", "v90", "v91", "v92", "v93", "v94", "v95", "v96", "v97", "v98", "v99", "v100", "v101", "v102", "v103", "v104", "v105", "v106", "v107", "v108", "v109", "v110", "v111", "v112", "v113", "v114", "v115", "v116", "v117", "v118", "v119", "v120", "v121", "v122", "v123", "v124", "v125", "v126", "v127", "v128", "v129", "v130", "v131", "v132", "v133", "v134", "v135", "v136", "v137", "v138", "v139", "v140", "v141", "v142", "v143", "v144", "v145", "v146", "v147", "v148", "v149", "v150", "v151", "v152", "v153", "v154", "v155", "v156", "v157", "v158", "v159", "v160", "v161", "v162", "v163", "v164", "v165", "v166", "v167", "v168", "v169", "v170", "v171", "v172", "v173", "v174", "v175", "v176", "v177", "v178", "v179", "v180", "v181", "v182", "v183", "v184", "v185", "v186", "v187", "v188", "v189", "v190", "v191", "v192", "v193", "v194", "v195", "v196", "v197", "v198", "v199", "v200", "v201", "v202", "v203", "v204", "v205", "v206", "v207", "v208", "v209", "v210", "v211", "v212", "v213", "v214", "v215", "v216", "v217", "v218", "v219", "v220", "v221", "v222", "v223", "v224", "v225", "v226", "v227", "v228", "v229", "v230", "v231", "v232", "v233", "v234", "v235", "v236", "v237", "v238", "v239", "v240", "v241", "v242", "v243", "v244", "v245", "v246", "v247", "v248", "v249", "v250", "a0", "a1", "a2", "a3", "a4", "a5", "a6", "a7", "a8", "a9", "a10", "a11", "a12", "a13", "a14", "a15", "a16", "a17", "a18", "a19", "a20", "a21", "a22", "a23", "a24", "a25", "a26", "a27", "a28", "a29", "a30", "a31", "a32", "a33", "a34", "a35", "a36", "a37", "a38", "a39", "a40", "a41", "a42", "a43", "a44", "a45", "a46", "a47", "a48", "a49", "a50", "a51", "a52", "a53", "a54", "a55", "a56", "a57", "a58", "a59", "a60", "a61", "a62", "a63", "a64", "a65", "a66", "a67", "a68", "a69", "a70", "a71", "a72", "a73", "a74", "a75", "a76", "a77", "a78", "a79", "a80", "a81", "a82", "a83", "a84", "a85", "a86", "a87", "a88", "a89", "a90", "a91", "a92", "a93", "a94", "a95", "a96", "a97", "a98", "a99", "a100", "a101", "a102", "a103", "a104", "a105", "a106", "a107", "a108", "a109", "a110", "a111", "a112", "a113", "a114", "a115", "a116", "a117", "a118", "a119", "a120", "a121", "a122", "a123", "a124", "a125", "a126", "a127", "a128", "a129", "a130", "a131", "a132", "a133", "a134", "a135", "a136", "a137", "a138", "a139", "a140", "a141", "a142", "a143", "a144", "a145", "a146", "a147", "a148", "a149", "a150", "a151", "a152", "a153", "a154", "a155", "a156", "a157", "a158", "a159", "a160", "a161", "a162", "a163", "a164", "a165", "a166", "a167", "a168", "a169", "a170", "a171", "a172", "a173", "a174", "a175", "a176", "a177", "a178", "a179", "a180", "a181", "a182", "a183", "a184", "a185", "a186", "a187", "a188", "a189", "a190", "a191", "a192", "a193", "a194", "a195", "a196", "a197", "a198", "a199", "a200", "a201", "a202", "a203", "a204", "a205", "a206", "a207", "a208", "a209", "a210", "a211", "a212", "a213", "a214", "a215", "a216", "a217", "a218", "a219", "a220", "a221", "a222", "a223", "a224", "a225", "a226", "a227", "a228", "a229", "a230", "a231", "a232", "a233", "a234", "a235", "a236", "a237", "a238", "a239", "a240", "a241", "a242", "a243", "a244", "a245", "a246", "a247", "a248", "a249", "a250", "a251", "a252", "a253", "a254", "a255", "s14", "s15", "s16", "s17", "s19", "s20", "s21", "s22", "s23", "s24", "s25", "s26", "s27", "s28", "s29", "s30", "s31", "s33", "s34", "s35", "s36", "s37", "s38", "s39", "s40", "s41", "s42", "s43", "s44", "s45", "s46", "s47", "s48", "s49", "s50", "s51", "s52", "s53", "s54", "s55", "s56", "s57", "s58", "s59", "vcc", "scc", "memory");
  if (bad) {
    const int lane = tid & 63, w = tid >> 6;
#pragma unroll 1
    for (int i = 0; i < ((b & 9) ? 3 : 4); ++i) {
      const unsigned xo = i == 3 ? x6 : (i == 0 ? x3 : (i == 1 ? x4 : x5)) + (lane & 1) * 128;
      slow_block(x, ws, out, xo, lane, i < 3 ? 7 : (1 << w));
    }
  }
}

extern "C" void kernel_launch(void* const* d_in, const int* in_sizes, int n_in, void* d_out, int out_size, void* d_ws, size_t ws_size, hipStream_t stream) {
  (void)hipFuncSetAttribute((const void*)fused_attn, hipFuncAttributeMaxDynamicSharedMemorySize, LDS_BYTES);
  fused_attn<<<256, 256, LDS_BYTES, stream>>>((const char*)d_in[0], (char*)d_out, (char*)d_ws, (const char*)d_in[1], (const char*)d_in[2], (const char*)d_in[3]);
}
```
